```python
import torch

def _softmax(x, axis=-1):
    return torch.softmax(x, dim=axis)
def _gelu(x):
    return torch.nn.functional.gelu(x, approximate="tanh")
def _silu(x):
    return torch.nn.functional.silu(x)
def _sigmoid(x):
    return torch.sigmoid(x)
def _layernorm(x, axis=-1, eps=1e-5):
    m = x.mean(dim=axis, keepdim=True)
    v = x.var(dim=axis, keepdim=True, unbiased=False)
    return (x - m) / torch.sqrt(v + eps)
def _scatter_add(tgt, idx, src, axis=0):
    return tgt.clone().index_add_(axis, idx, src)
def _segment_sum(src, seg, n):
    out = torch.zeros((n,) + src.shape[1:], dtype=src.dtype)
    return out.index_add_(0, seg, src)
def _pad(x, pw):
    flat = tuple(v for pair in reversed(pw) for v in pair)
    return torch.nn.functional.pad(x, flat)

kernel_name = 'interleave_layers__sliding_block__dense_activation_gelu__prompt'

def setup_inputs():

    _w = (lambda a: (lambda t: t.to(torch.bfloat16) if t.is_floating_point() else t)(torch.as_tensor(a)))
    def _A_setup():
        B, S, D = 1, 2048, 512
        return {
            "x": _w((lambda *s: torch.randn(*s, dtype=torch.bfloat16) * 0.05)(B, S, D)),
            "wqkv": _w((lambda *s: torch.randn(*s, dtype=torch.bfloat16) * 0.05)(D, 3 * D)),
            "wo": _w((lambda *s: torch.randn(*s, dtype=torch.bfloat16) * 0.05)(D, D)),
        }
    _A_in = _A_setup()

    def _B_setup():
        B, S, D, F = 1, 2048, 512, 1792
        return {
            "x": _w((lambda *s: torch.randn(*s, dtype=torch.bfloat16) * 0.05)(B, S, D)),
            "w1": _w((lambda *s: torch.randn(*s, dtype=torch.bfloat16) * 0.8)(D, F) * (D ** -0.5)),
            "b1": _w((lambda *s: torch.randn(*s, dtype=torch.bfloat16) * 0.8)(F) * 0.01),
            "w2": _w((lambda *s: torch.randn(*s, dtype=torch.bfloat16) * 0.8)(F, D) * (F ** -0.5)),
            "b2": _w((lambda *s: torch.randn(*s, dtype=torch.bfloat16) * 0.8)(D) * 0.01),
        }
    _B_in = _B_setup()

    return {
        "A_x": _A_in["x"],
        "A_wqkv": _A_in["wqkv"],
        "A_wo": _A_in["wo"],
        "B_x": _B_in["x"],
        "B_w1": _B_in["w1"],
        "B_b1": _B_in["b1"],
        "B_w2": _B_in["w2"],
        "B_b2": _B_in["b2"],
    }

def reference(A_x, A_wqkv, A_wo, B_x, B_w1, B_b1, B_w2, B_b2):
    def _A_ref(x, wqkv, wo):
        B, S, D = x.shape
        H = 32; Dh = D // H; W = 256
        qkv = (lambda x, *d: x.permute(*d))((x @ wqkv).reshape(B, S, 3, H, Dh), 2, 0, 3, 1, 4)
        q, k, v = qkv[0], qkv[1], qkv[2]
        scores = torch.einsum("bhqd,bhkd->bhqk", q, k) * (Dh ** -0.5)
        idx = torch.arange(S)
        mask = (idx[:, None] - idx[None, :] >= 0) & (idx[:, None] - idx[None, :] < W)
        scores = torch.where(mask[None, None], scores, -1e9)
        p = _softmax(scores, axis=-1)
        o = torch.einsum("bhqk,bhkd->bhqd", p, v)
        return (lambda x, *d: x.permute(*d))(o, 0, 2, 1, 3).reshape(B, S, D) @ wo

    def _B_ref(x, w1, b1, w2, b2):
        h = torch.einsum("bsd,df->bsf", x, w1) + b1
        h = _gelu(h)
        return torch.einsum("bsf,fd->bsd", h, w2) + b2

    x = A_x
    x = x + _A_ref(_layernorm(x, axis=-1), A_wqkv, A_wo)
    x = x + _B_ref(_layernorm(x, axis=-1), B_w1, B_b1, B_w2, B_b2)
    x = x + _A_ref(_layernorm(x, axis=-1), A_wqkv, A_wo)
    x = x + _B_ref(_layernorm(x, axis=-1), B_w1, B_b1, B_w2, B_b2)
    return x
```

```cpp
#include <hip/hip_runtime.h>
#include <hip/hip_bf16.h>
#include <stdint.h>

typedef unsigned short u16;
__device__ __forceinline__ float bf2f(u16 b){ return __builtin_bit_cast(float, (unsigned)b << 16); }
__device__ __forceinline__ u16 f2bf(float f){ unsigned u = __builtin_bit_cast(unsigned, f); u += 0x7FFFu + ((u>>16)&1u); return (u16)(u>>16); }

constexpr int S=2048, D=512, H=32, DH=16, W=256, F=1792;

__global__ void ln_kernel(const u16* x, u16* y){
  int row = blockIdx.x; int t = threadIdx.x;
  float v[8]; float s=0.f;
  for(int i=0;i<8;i++){ v[i]=bf2f(x[row*D + t*8+i]); s+=v[i]; }
  for(int o=32;o>0;o>>=1) s += __shfl_xor(s,o);
  float m = s/D; float q=0.f;
  for(int i=0;i<8;i++){ float d=v[i]-m; q+=d*d; }
  for(int o=32;o>0;o>>=1) q += __shfl_xor(q,o);
  float r = rsqrtf(q/D + 1e-5f);
  for(int i=0;i<8;i++) y[row*D+t*8+i] = f2bf((v[i]-m)*r);
}
__global__ void gemm_naive(const u16* A, const u16* B, u16* C, const u16* bias, const u16* res, int N, int K, int mode){
  int n = blockIdx.x*64 + (threadIdx.x&63); int m = blockIdx.y*4 + (threadIdx.x>>6);
  float acc=0.f;
  for(int k=0;k<K;k++) acc += bf2f(A[m*K+k])*bf2f(B[k*N+n]);
  if(mode==1||mode==3) acc += bf2f(bias[n]);
  if(mode==1){ float u = 0.7978845608028654f*(acc+0.044715f*acc*acc*acc); acc = 0.5f*acc*(1.f+tanhf(u)); }
  if(mode>=2) acc += bf2f(res[m*N+n]);
  C[m*N+n]=f2bf(acc);
}
__global__ void attn_naive(const u16* qkv, u16* o){
  int q = blockIdx.x*64 + (threadIdx.x&63); int h = blockIdx.y;
  float qv[16]; for(int d=0;d<16;d++) qv[d]=bf2f(qkv[q*1536 + h*16+d])*0.25f;
  float m=-1e30f,l=0.f,acc[16]; for(int d=0;d<16;d++) acc[d]=0.f;
  int k0 = q-W+1; if(k0<0)k0=0;
  for(int k=k0;k<=q;k++){
    float s=0.f; for(int d=0;d<16;d++) s+=qv[d]*bf2f(qkv[k*1536+512+h*16+d]);
    float mn=fmaxf(m,s); float a=__expf(m-mn), p=__expf(s-mn);
    l=l*a+p; for(int d=0;d<16;d++) acc[d]=acc[d]*a+p*bf2f(qkv[k*1536+1024+h*16+d]);
    m=mn;
  }
  for(int d=0;d<16;d++) o[q*512+h*16+d]=f2bf(acc[d]/l);
}

extern "C" void kernel_launch(void* const* d_in, const int* in_sizes, int n_in,
                              void* d_out, int out_size, void* d_ws, size_t ws_size,
                              hipStream_t stream) {
  const u16* x=(const u16*)d_in[0]; const u16* wqkv=(const u16*)d_in[1]; const u16* wo=(const u16*)d_in[2];
  const u16* w1=(const u16*)d_in[4]; const u16* b1=(const u16*)d_in[5]; const u16* w2=(const u16*)d_in[6]; const u16* b2=(const u16*)d_in[7];
  u16* ws=(u16*)d_ws;
  u16* xn=ws; u16* qkv=xn+S*D; u16* o=qkv+S*1536; u16* x1=o+S*D; u16* hh=x1+S*D; u16* x2=hh+S*F;
  u16* out=(u16*)d_out;
  const u16* cur=x;
  for(int l=0;l<2;l++){
    ln_kernel<<<S,64,0,stream>>>(cur,xn);
    gemm_naive<<<dim3(1536/64,S/4),256,0,stream>>>(xn,wqkv,qkv,nullptr,nullptr,1536,512,0);
    attn_naive<<<dim3(S/64,H),64,0,stream>>>(qkv,o);
    gemm_naive<<<dim3(512/64,S/4),256,0,stream>>>(o,wo,x1,nullptr,cur,512,512,2);
    ln_kernel<<<S,64,0,stream>>>(x1,xn);
    gemm_naive<<<dim3(F/64,S/4),256,0,stream>>>(xn,w1,hh,b1,nullptr,F,512,1);
    u16* dst = l==0? x2: out;
    gemm_naive<<<dim3(512/64,S/4),256,0,stream>>>(hh,w2,dst,b2,x1,512,F,3);
    cur=dst;
  }
}
```

```cpp
#include <hip/hip_runtime.h>
#include <stdint.h>
#include <type_traits>

namespace {
#define DI __device__ __forceinline__
#define GAS __attribute__((address_space(1)))
#define LAS __attribute__((address_space(3)))
#define CAS __attribute__((address_space(4)))
typedef unsigned v4u __attribute__((ext_vector_type(4)));
typedef unsigned v2u __attribute__((ext_vector_type(2)));
typedef float f32x16 __attribute__((ext_vector_type(16)));
typedef float f32x4 __attribute__((ext_vector_type(4)));
typedef float f32x2 __attribute__((ext_vector_type(2)));
typedef __bf16 bf16x8 __attribute__((ext_vector_type(8)));
typedef __bf16 bf16x4 __attribute__((ext_vector_type(4)));
typedef __bf16 bf16x2 __attribute__((ext_vector_type(2)));
typedef unsigned long long u64;
typedef GAS u64 gu64;
typedef __amdgpu_buffer_rsrc_t rsrc_t;
#define RLX_AGENT __ATOMIC_RELAXED, __HIP_MEMORY_SCOPE_AGENT

constexpr unsigned MB = 1u << 20;
constexpr unsigned OFF_EPOCH = 0, OFF_HELLO = 4096, OFF_FKV = 8192, OFF_MBX = 8192 + 65536;
constexpr unsigned OFF_DBG = 512 * 1024;
constexpr unsigned OFF_ST = 640 * 1024;
constexpr unsigned OFF_WOT = 1 * MB;
constexpr unsigned OFF_W1T = OFF_WOT + 512 * 1024;
constexpr unsigned OFF_W2T = OFF_W1T + 1792 * 1024;
constexpr unsigned OFF_X2 = OFF_W2T + 1792 * 1024;
constexpr unsigned OFF_X1 = OFF_X2 + 2 * MB;
constexpr unsigned OFF_O = OFF_X1 + 4 * MB;
constexpr unsigned OFF_KV = OFF_O + 4 * MB;
constexpr unsigned OFF_H = OFF_KV + 8 * MB;
constexpr unsigned WS_BYTES = OFF_H + 14 * MB;
constexpr unsigned LA = 0, LB = 65536, LC = 131072, LT = 147456, LDS_BYTES = 163840;
constexpr unsigned T_CSQKV = LT;
constexpr unsigned T_CS1 = LT + 512;
constexpr unsigned T_B1 = LT + 1024;
constexpr unsigned T_STAT = LT + 2048;
constexpr unsigned T_B2 = T_B1 + 512;
constexpr unsigned T_JUNK = LT + 4096;
constexpr unsigned SPIN_CAP = 1u << 18;

struct Params {
  const unsigned char* x; const unsigned char* wqkv; const unsigned char* wo; const unsigned char* w1;
  const unsigned char* b1; const unsigned char* w2; const unsigned char* b2; unsigned char* out; unsigned char* ws;
};

DI float asf(unsigned u) { return __builtin_bit_cast(float, u); }
DI unsigned asu(float f) { return __builtin_bit_cast(unsigned, f); }
DI bf16x2 asb2(unsigned u) { return __builtin_bit_cast(bf16x2, u); }
DI rsrc_t mk_rsrc(const void* p, unsigned bytes) { return __builtin_amdgcn_make_buffer_rsrc((void*)p, 0, bytes, 0x00020000); }
DI v4u ld128(rsrc_t r, unsigned voff, unsigned soff) { return __builtin_amdgcn_raw_buffer_load_b128(r, voff, soff, 0); }
DI v4u ld128c(rsrc_t r, unsigned voff, unsigned soff) { return __builtin_amdgcn_raw_buffer_load_b128(r, voff, soff, 16); }
DI v2u ld64c(rsrc_t r, unsigned voff, unsigned soff) { return __builtin_amdgcn_raw_buffer_load_b64(r, voff, soff, 16); }
DI void st128c(v4u v, rsrc_t r, unsigned voff, unsigned soff) { __builtin_amdgcn_raw_buffer_store_b128(v, r, voff, soff, 16); }
DI void st64c(v2u v, rsrc_t r, unsigned voff, unsigned soff) { __builtin_amdgcn_raw_buffer_store_b64(v, r, voff, soff, 16); }
#define LDF(r, v16, sbase, i) ld128c(r, (v16) + ((i) & 3) * 1024u, (sbase) + ((i) >> 2) * 4096u)

DI void dma(rsrc_t r, unsigned voff, unsigned soff, unsigned lds) {
  asm volatile("s_mov_b32 m0, %3\n\ts_nop 0\n\tbuffer_load_dwordx4 %0, %1, %2 offen lds" ::"v"(voff), "s"(r), "s"(soff), "s"(lds));
}
DI void dmac(rsrc_t r, unsigned voff, unsigned soff, unsigned lds) {
  asm volatile("s_mov_b32 m0, %3\n\ts_nop 0\n\tbuffer_load_dwordx4 %0, %1, %2 offen sc1 lds" ::"v"(voff), "s"(r), "s"(soff), "s"(lds));
}
DI void dmac4(rsrc_t r, unsigned voff, unsigned soff, unsigned lds) {
  asm volatile("s_mov_b32 m0, %3\n\ts_nop 0\n\tbuffer_load_dwordx4 %0, %1, %2 offen sc1 lds\n\tbuffer_load_dwordx4 %0, %1, %2 offen offset:1024 sc1 lds\n\t"
               "buffer_load_dwordx4 %0, %1, %2 offen offset:2048 sc1 lds\n\tbuffer_load_dwordx4 %0, %1, %2 offen offset:3072 sc1 lds" ::"v"(voff), "s"(r), "s"(soff), "s"(lds));
}
#define WAIT_VM0() asm volatile("s_waitcnt vmcnt(0)" ::: "memory")
#define BAR() asm volatile("s_waitcnt lgkmcnt(0)\n\ts_barrier" ::: "memory")
#define SB() __builtin_amdgcn_sched_barrier(0)

DI f32x16 mfma(v4u a, v4u b, f32x16 c) {
  return __builtin_amdgcn_mfma_f32_32x32x16_bf16(__builtin_bit_cast(bf16x8, a), __builtin_bit_cast(bf16x8, b), c, 0, 0, 0);
}
DI v4u trread(LAS unsigned char* p) {
  bf16x4 a = __builtin_amdgcn_ds_read_tr16_b64_v4bf16((LAS bf16x4*)p);
  bf16x4 b = __builtin_amdgcn_ds_read_tr16_b64_v4bf16((LAS bf16x4*)(p + 256));
  return __builtin_bit_cast(v4u, __builtin_shufflevector(a, b, 0, 1, 2, 3, 4, 5, 6, 7));
}
DI v4u lds128(LAS unsigned char* p) { return *(LAS v4u*)p; }
DI f32x4 ldsf4(LAS unsigned char* p) { return *(LAS f32x4*)p; }
DI unsigned pk(float a, float b) { f32x2 v = {a, b}; return __builtin_bit_cast(unsigned, __builtin_convertvector(v, bf16x2)); }
DI float bflo(unsigned u) { return __builtin_bit_cast(float, u << 16); }
DI float bfhi(unsigned u) { return __builtin_bit_cast(float, u & 0xffff0000u); }
DI float xhalf_add(float v) { auto r = __builtin_amdgcn_permlane32_swap(__builtin_bit_cast(unsigned, v), __builtin_bit_cast(unsigned, v), false, false);
  return asf(r[0]) + asf(r[1]); }
DI float xhalf_max(float v) { auto r = __builtin_amdgcn_permlane32_swap(__builtin_bit_cast(unsigned, v), __builtin_bit_cast(unsigned, v), false, false);
  return fmaxf(asf(r[0]), asf(r[1])); }
DI float x16_add(float v) { auto r = __builtin_amdgcn_permlane16_swap(__builtin_bit_cast(unsigned, v), __builtin_bit_cast(unsigned, v), false, false);
  return asf(r[0]) + asf(r[1]); }
DI float x8_add(float v) { return v + __builtin_bit_cast(float, __builtin_amdgcn_update_dpp(0, __builtin_bit_cast(int, v), 0x128, 0xf, 0xf, false)); }
DI f32x16 zero16() { f32x16 z; for (int i = 0; i < 16; ++i) z[i] = 0.f; return z; }

DI void poll(rsrc_t r, unsigned voff, unsigned soff, bool active, unsigned tlo, unsigned thi) {
  for (unsigned spins = 0; spins < SPIN_CAP; ++spins) {
    asm volatile("" ::: "memory");
    const v2u f = ld64c(r, active ? voff : 0x80000000u, soff);
    if (__all(!active || (f[0] == tlo && f[1] == thi))) break;
  }
  asm volatile("" ::: "memory");
}
DI void post(rsrc_t r, unsigned voff, unsigned soff, bool active, unsigned tlo, unsigned thi) { st64c(v2u{tlo, thi}, r, active ? voff : 0x80000000u, soff); }

#define ST(i)

DI float gelu(float x) {
  const float c0 = -2.f * 1.4426950408889634f * 0.7978845608028654f, c1 = c0 * 0.044715f;
  float z = x * __builtin_fmaf(x * x, c1, c0);
  float e = __builtin_amdgcn_exp2f(z);
  return x * __builtin_amdgcn_rcpf(1.f + e);
}

template <int I, int N, class F> DI void static_for(F&& f) { if constexpr (I < N) { f(std::integral_constant<int, I>{}); static_for<I + 1, N>(f); } }
DI float ex2(float x) { return __builtin_amdgcn_exp2f(x); }
DI float max3(float a, float b, float c) { return fmaxf(fmaxf(a, b), c); }

template <int HD, class Hook>
DI v4u attn_unit(const v4u (&kf)[9], v4u q, const v4u (&vf)[18], const unsigned (&pm)[8], int lane, float cnt, Hook&& hook) {
  const int qi = lane & 31, h4 = (lane >> 5) * 4;
  const f32x16 z = zero16();
  const v4u ones = {0x3f803f80u, 0x3f803f80u, 0x3f803f80u, 0x3f803f80u};
  f32x16 c;
  {
    f32x16 sd = mfma(kf[8], q, z), sf = mfma(kf[0], q, z), s = mfma(kf[1], q, z);
    SB();
    float m0 = -3.0e38f, m1 = -3.0e38f;
#pragma unroll
    for (int r = 0; r < 16; r += 2) {
      const int i0 = (r & 3) + 8 * (r >> 2);
      m0 = max3(m0, (i0 + h4 <= qi) ? sd[r] : sf[r], (i0 + 1 + h4 <= qi) ? sd[r + 1] : sf[r + 1]);
    }
    f32x16 s2 = mfma(kf[2], q, z);
    SB();
#pragma unroll
    for (int d = 1; d < 8; ++d) {
      f32x16 sn = s2;
      if (d + 2 < 8) sn = mfma(kf[d + 2], q, z);
      SB();
#pragma unroll
      for (int r = 0; r < 16; r += 4) { m1 = max3(m1, s[r], s[r + 1]); m0 = max3(m0, s[r + 2], s[r + 3]); }
      s = s2; s2 = sn;
      SB();
    }
    const float m = xhalf_max(fmaxf(m0, m1));
#pragma unroll
    for (int i = 0; i < 16; ++i) c[i] = -m;
  }
  f32x16 o, l;
  float p0;
  v4u ph;
  f32x16 s;
  {
    f32x16 sd = mfma(kf[8], q, c), sf = mfma(kf[0], q, c);
    s = mfma(kf[1], q, c);
    SB();
    hook(std::integral_constant<int, 8>{}); hook(std::integral_constant<int, 0>{}); hook(std::integral_constant<int, 1>{});
    unsigned p[8];
#pragma unroll
    for (int r = 0; r < 8; r += 2) {
      const int i0 = (r & 3) + 8 * (r >> 2);
      p[r >> 1] = pk(ex2((i0 + h4 <= qi) ? sd[r] : sf[r]), ex2((i0 + 1 + h4 <= qi) ? sd[r + 1] : sf[r + 1]));
    }
    p0 = bflo(pk(ex2(fminf(sf[0], 0.f)), 0.f));
    SB();
    l = mfma(ones, v4u{p[0], p[1], p[2], p[3]}, z);
    const v4u pd0 = {p[0] & pm[0], p[1] & pm[1], p[2] & pm[2], p[3] & pm[3]}, pf0 = {p[0] & ~pm[0], p[1] & ~pm[1], p[2] & ~pm[2], p[3] & ~pm[3]};
    SB();
    o = mfma(vf[16], pd0, z);
#pragma unroll
    for (int r = 8; r < 12; r += 2) {
      const int i0 = (r & 3) + 8 * (r >> 2);
      p[r >> 1] = pk(ex2((i0 + h4 <= qi) ? sd[r] : sf[r]), ex2((i0 + 1 + h4 <= qi) ? sd[r + 1] : sf[r + 1]));
    }
    SB();
    o = mfma(vf[0], pf0, o);
#pragma unroll
    for (int r = 12; r < 16; r += 2) {
      const int i0 = (r & 3) + 8 * (r >> 2);
      p[r >> 1] = pk(ex2((i0 + h4 <= qi) ? sd[r] : sf[r]), ex2((i0 + 1 + h4 <= qi) ? sd[r + 1] : sf[r + 1]));
    }
    SB();
    l = mfma(ones, v4u{p[4], p[5], p[6], p[7]}, l);
    const v4u pd1 = {p[4] & pm[4], p[5] & pm[5], p[6] & pm[6], p[7] & pm[7]};
    ph = v4u{p[4] & ~pm[4], p[5] & ~pm[5], p[6] & ~pm[6], p[7] & ~pm[7]};
    SB();
    o = mfma(vf[17], pd1, o);
    SB();
  }
  static_for<1, 8>([&](auto D_) {
    constexpr int d = D_.value;
    float e[16];
    f32x16 sn = s;
    e[0] = ex2(s[0]); e[1] = ex2(s[1]); e[2] = ex2(s[2]);
    SB();
    o = mfma(vf[2 * d - 1], ph, o);
    e[3] = ex2(s[3]); e[4] = ex2(s[4]); e[5] = ex2(s[5]);
    SB();
    if constexpr (d > 1) l = mfma(ones, ph, l);
    e[6] = ex2(s[6]); e[7] = ex2(s[7]);
    const v4u pl = {pk(e[0], e[1]), pk(e[2], e[3]), pk(e[4], e[5]), pk(e[6], e[7])};
    SB();
    if constexpr (d + 1 < 8) { sn = mfma(kf[d + 1], q, c); hook(std::integral_constant<int, d + 1>{}); }
    e[8] = ex2(s[8]); e[9] = ex2(s[9]); e[10] = ex2(s[10]);
    SB();
    o = mfma(vf[2 * d], pl, o);
    e[11] = ex2(s[11]); e[12] = ex2(s[12]); e[13] = ex2(s[13]);
    SB();
    l = mfma(ones, pl, l);
    e[14] = ex2(s[14]); e[15] = ex2(s[15]);
    ph = v4u{pk(e[8], e[9]), pk(e[10], e[11]), pk(e[12], e[13]), pk(e[14], e[15])};
    s = sn;
    SB();
  });
  l = mfma(ones, ph, l);
  o = mfma(vf[15], ph, o);
  const float inv = __builtin_amdgcn_rcpf(__builtin_fmaf(-cnt, p0, l[0]));
  v4u r;
#pragma unroll
  for (int i = 0; i < 4; ++i) r[i] = pk(o[8 * HD + 2 * i] * inv, o[8 * HD + 2 * i + 1] * inv);
  return r;
}

#define CTX() \
  int b = b0, w = w0, lane = lane0; unsigned tlo = tag_lo, thi = tag_hi; const CAS Params* P = P0; \
  asm volatile("" : "+s"(b), "+s"(w), "+s"(tlo), "+s"(thi), "+s"(P)); asm volatile("" : "+v"(lane)); \
  const int tid = w * 64 + lane, pn = 2 * (b & 7) + (b >> 7), j = (b >> 3) & 15, cu = pn * 16 + j, h = lane >> 5, tok = lane & 31; \
  const unsigned v16 = lane * 16; const int th = j & 1, ng = j >> 1, ft = lane & 7, fc = lane >> 3, gtb = pn * 4 + w; \
  const unsigned trb = (8 * h + ((lane & 15) >> 2)) * 64 + ((lane >> 4) & 1) * 32 + (lane & 3) * 8; \
  const unsigned vq = (lane >> 2) * 3072 + (lane & 3) * 16; \
  (void)tid; (void)cu; (void)tok; (void)v16; (void)th; (void)ng; (void)ft; (void)fc; (void)gtb; (void)trb; (void)vq;

template <int I> DI void w2_load(rsrc_t r, unsigned voff, unsigned soff) {
  asm volatile("buffer_load_dwordx4 a[%c3:%c4], %0, %1, %2 offen offset:%c5 sc1" ::"v"(voff), "s"(r), "s"(soff), "i"(4 * I), "i"(4 * I + 3), "i"((I & 3) * 1024));
}
template <int LO, int HI> DI void w2_load_range(rsrc_t r, unsigned voff, unsigned sbase) {
  if constexpr (LO < HI) { w2_load<LO>(r, voff, sbase + (LO >> 2) * 4096); w2_load_range<LO + 1, HI>(r, voff, sbase); }
}
template <int I, bool FIRST> DI void w2_mfma(f32x16& acc, v4u x) {
  if constexpr (FIRST) asm volatile("v_mfma_f32_32x32x16_bf16 %0, a[%c2:%c3], %1, 0" : "=&v"(acc) : "v"(x), "i"(4 * I), "i"(4 * I + 3));
  else asm volatile("v_mfma_f32_32x32x16_bf16 %0, a[%c2:%c3], %1, %0" : "+v"(acc) : "v"(x), "i"(4 * I), "i"(4 * I + 3));
}

extern "C" __global__ void __launch_bounds__(256, 2) fused_body(Params P_) {
  extern __shared__ __attribute__((aligned(16))) unsigned char smem_[];
  LAS unsigned char* lds = (LAS unsigned char*)smem_;
  const int lane0 = threadIdx.x & 63, w0 = __builtin_amdgcn_readfirstlane(threadIdx.x >> 6), b0 = blockIdx.x;
  const CAS Params* P0 = (const CAS Params*)__builtin_amdgcn_kernarg_segment_ptr();
  const rsrc_t rW = mk_rsrc(P0->ws, WS_BYTES);
  int L = 0;
  ST(0);
  unsigned tag_lo = 0, tag_hi = 0;
  v4u res[2];
  const v4u ones = {0x3f803f80u, 0x3f803f80u, 0x3f803f80u, 0x3f803f80u};
  constexpr int WIN = 10;
  v4u x[32];
  f32x16 aK = zero16(), aV = zero16();
  float rstd, mur;
  {
    CTX();
    const v2u e = ld64c(rW, 0, OFF_EPOCH);
    const int gw = b * 4 + w, rho = lane >> 2, c = lane & 3;
    const int kperm = ((rho >> 2) & 1) * 8 + (rho >> 3) * 4 + (rho & 3);
    const rsrc_t rW2 = mk_rsrc(P->w2, 1792 * 512 * 2), rW1 = mk_rsrc(P->w1, 512 * 1792 * 2), rWO = mk_rsrc(P->wo, 512 * 512 * 2);
    const rsrc_t rX = mk_rsrc(P->x, 2048 * 512 * 2), rQ = mk_rsrc(P->wqkv, 512 * 1536 * 2);
    const unsigned sc = LB + 32768 + w * 4096;
    const unsigned vperm = kperm * 1024 + c * 16, vnat1 = rho * 3584 + c * 16, vhalf = (rho + 16 * (c >> 1)) * 3584 + (c & 1) * 16;
    { int f = gw; dma(rW2, vperm, ((f % 224) >> 1) * 16384 + ((f / 224) * 2 + (f & 1)) * 64, sc); }
    if (gw < 768) { int f = 1024 + gw; dma(rW2, vperm, ((f % 224) >> 1) * 16384 + ((f / 224) * 2 + (f & 1)) * 64, sc + 1024); }
    else { int f = gw - 768; dma(rW1, vhalf, (f & 15) * 32 * 3584 + ((f >> 4) * 112 + 96) * 2, sc + 1024); }
    { int f = gw; dma(rW1, vnat1, (f & 31) * 16 * 3584 + ((f / 96) * 112 + ((f % 96) >> 5) * 32) * 2, sc + 2048); }
    if (gw < 512) { int f = 1024 + gw; dma(rW1, vnat1, (f & 31) * 16 * 3584 + ((f / 96) * 112 + ((f % 96) >> 5) * 32) * 2, sc + 3072); }
    else { int f = gw - 512; dma(rWO, vperm, ((f & 63) >> 1) * 16384 + ((f >> 6) * 2 + (f & 1)) * 64, sc + 3072); }
    {
      u64 pc; int len;
      asm volatile("s_getpc_b64 %0\n.Lfused_pc:\n\ts_mov_b32 %1, .Lfused_end-.Lfused_pc" : "=s"(pc), "=s"(len));
      asm volatile("s_mov_b32 m0, %2\n\ts_nop 0\n\tbuffer_load_dword %0, %1, 0 offen lds" ::"v"((unsigned)(((b >> 3) * 4 + w) * 1024 + (lane & 7) * 128)),
                   "s"(mk_rsrc((const void*)pc, len > 0 ? len : 0)), "s"(T_JUNK + w * 256));
    }
#pragma unroll
    for (int i = 0; i < 8; ++i) { const int s = w * 8 + i;
      dma(rQ, vq, s * 16 * 3072 + 1024 + j * 64, LA + s * 1024); dma(rQ, vq, s * 16 * 3072 + 2048 + j * 64, LA + 32768 + s * 1024); }
#pragma unroll
    for (int i = 0; i < 8; ++i) { const int s = w * 8 + i; dma(rQ, vq, s * 16 * 3072 + j * 64, LB + s * 1024); }
#pragma unroll
    for (int t = 0; t < 2; ++t) res[t] = ld128(rX, (unsigned)(ft * 1024 + fc * 16), (unsigned)(((pn * 4 + th * 2 + t) * 32 + w * 8) * 1024 + ng * 128));
    const unsigned char *pb1 = P->b1, *pb2 = P->b2;
    const unsigned bt = __builtin_amdgcn_raw_buffer_load_b16(mk_rsrc(w < 2 ? pb1 : pb2, w < 2 ? 3584 : 1024), lane * 2, w < 2 ? j * 224 + w * 128 : ng * 128, 0);
        const unsigned vx = tok * 1024 + h * 16, sx = gtb * 32768;
#pragma unroll
        for (int s = 0; s < WIN; ++s) x[s] = ld128(rX, vx + s * 32, sx);
        asm volatile("s_waitcnt vmcnt(%0)" ::"n"(WIN) : "memory");
        const u64 tag = ((u64)(unsigned)__builtin_amdgcn_readfirstlane(e[1]) << 32 | (unsigned)__builtin_amdgcn_readfirstlane(e[0])) + 1;
        tag_lo = (unsigned)tag; tag_hi = (unsigned)(tag >> 32);
        *(LAS float*)(lds + T_B1 + tid * 4) = bflo(bt);
        {
          LAS unsigned char* sc = lds + LB + 32768 + w * 4096 + trb;
          v4u t0 = trread(sc), t1 = trread(sc + 1024), t2 = trread(sc + 2048), t3 = trread(sc + 3072);
          st128c(t0, rW, v16, OFF_W2T + gw * 1024);
          st128c(t1, rW, v16, gw < 768 ? OFF_W2T + (1024 + gw) * 1024 : OFF_W1T + (((gw - 768) >> 4) * 112 + 96 + ((gw - 768) & 15)) * 1024);
          st128c(t2, rW, v16, OFF_W1T + ((gw / 96) * 112 + gw % 96) * 1024);
          st128c(t3, rW, v16, gw < 512 ? OFF_W1T + (((1024 + gw) / 96) * 112 + (1024 + gw) % 96) * 1024 : OFF_WOT + (gw - 512) * 1024);
        }
        BAR();
        ST(1);
        f32x16 aC = zero16();
        float s0 = 0.f, s1 = 0.f, q0 = 0.f, q1 = 0.f;
        LAS unsigned char* pa = lds + LA + trb;
        LAS unsigned char* pc = lds + (w == 0 ? LA : w == 1 ? LA + 32768 : LB) + trb;
        v4u wk = trread(pa), wv = trread(pa + 32768), wc = trread(pc);
        const bf16x2 one2 = __builtin_bit_cast(bf16x2, 0x3f803f80u);
#pragma unroll
        for (int s = 0; s < 32; ++s) {
          if (s + WIN < 32) x[s + WIN] = ld128(rX, vx + (s + WIN) * 32, sx);
          v4u nk = wk, nv = wv, nc = wc;
          if (s + 1 < 32) { nk = trread(pa + (s + 1) * 1024); nv = trread(pa + 32768 + (s + 1) * 1024); nc = trread(pc + (s + 1) * 1024); }
          aC = mfma(wc, ones, aC);
          aK = mfma(wk, x[s], aK);
          aV = mfma(x[s], wv, aV);
#pragma unroll
          for (int i = 0; i < 4; i += 2) {
            bf16x2 u = asb2(x[s][i]), v = asb2(x[s][i + 1]);
            s0 = __builtin_amdgcn_fdot2_f32_bf16(u, one2, s0, false); s1 = __builtin_amdgcn_fdot2_f32_bf16(v, one2, s1, false);
            q0 = __builtin_amdgcn_fdot2_f32_bf16(u, u, q0, false); q1 = __builtin_amdgcn_fdot2_f32_bf16(v, v, q1, false);
          }
          asm volatile("" : "+v"(s0), "+v"(s1), "+v"(q0), "+v"(q1));
          wk = nk; wv = nv; wc = nc;
          SB();
        }
        if (tok == 0) {
#pragma unroll
          for (int g = 0; g < 4; ++g) *(LAS f32x4*)(lds + T_CSQKV + w * 128 + (8 * g + 4 * h) * 4) = f32x4{aC[4 * g], aC[4 * g + 1], aC[4 * g + 2], aC[4 * g + 3]};
        }
        const float S = xhalf_add(s0 + s1), Q = xhalf_add(q0 + q1);
        const float mean = S * (1.f / 512.f), var = fmaxf(Q * (1.f / 512.f) - mean * mean, 0.f);
        rstd = __builtin_amdgcn_rsqf(var + 1e-5f); mur = mean * rstd;
        BAR();

  }

#pragma unroll 1
  for (;;) {
    v4u qa, qb;
    {
      ST(2);
      CTX();
      {
        const unsigned skv = OFF_KV + L * 4 * MB + (j * 64 + gtb) * 4096;
        *(LAS float*)(lds + T_STAT + w * 256 + h * 128 + tok * 4) = h ? mur : rstd;
        v4u ka, kb, v0, v1;
        const float csv = *(LAS float*)(lds + T_CSQKV + 128 + tok * 4);
#pragma unroll
        for (int g = 0; g < 4; ++g) {
          const f32x4 cs = ldsf4(lds + T_CSQKV + (8 * g + 4 * h) * 4);
          float e[4];
#pragma unroll
          for (int i = 0; i < 4; ++i) e[i] = __builtin_fmaf(aK[4 * g + i], rstd, -mur * cs[i]);
          if (g < 2) { ka[2 * g] = pk(e[0], e[1]); ka[2 * g + 1] = pk(e[2], e[3]); } else { kb[2 * g - 4] = pk(e[0], e[1]); kb[2 * g - 3] = pk(e[2], e[3]); }
        }
        st128c(ka, rW, v16, skv); st128c(kb, rW, v16 + 1024, skv);
#pragma unroll
        for (int g = 0; g < 4; ++g) {
          const f32x4 rs = ldsf4(lds + T_STAT + w * 256 + (8 * g + 4 * h) * 4), mu = ldsf4(lds + T_STAT + w * 256 + 128 + (8 * g + 4 * h) * 4);
          float e[4];
#pragma unroll
          for (int i = 0; i < 4; ++i) e[i] = __builtin_fmaf(aV[4 * g + i], rs[i], -mu[i] * csv);
          if (g < 2) { v0[2 * g] = pk(e[0], e[1]); v0[2 * g + 1] = pk(e[2], e[3]); } else { v1[2 * g - 4] = pk(e[0], e[1]); v1[2 * g - 3] = pk(e[2], e[3]); }
        }
        st128c(v0, rW, v16 + 2048, skv); st128c(v1, rW, v16 + 3072, skv);
      }
      SB();
      {
        f32x16 aQ = zero16();
        LAS unsigned char* pb = lds + LB + trb;
        v4u wq = trread(pb), wq1 = trread(pb + 1024);
#pragma unroll
        for (int s = 0; s < 32; ++s) {
          v4u nq = wq1;
          if (s + 2 < 32) nq = trread(pb + (s + 2) * 1024);
          aQ = mfma(wq, x[s], aQ);
          wq = wq1; wq1 = nq;
          SB();
          if (s == 22) {
            WAIT_VM0();
            asm volatile("s_barrier" ::: "memory");
            if (w == 0) post(rW, 0, OFF_FKV + (L * 256 + cu) * 128, lane == 0, tlo, thi);
            if (w == 1 && L == 0) post(rW, 0, OFF_HELLO + b * 8, lane == 0, tlo, thi);
            SB();
          }
        }
        const float qs = 0.25f * 1.4426950408889634f, r2 = rstd * qs, m2 = mur * qs;
#pragma unroll
        for (int g = 0; g < 4; ++g) {
          const f32x4 cs = ldsf4(lds + T_CSQKV + 256 + (8 * g + 4 * h) * 4);
          float e[4];
#pragma unroll
          for (int i = 0; i < 4; ++i) e[i] = __builtin_fmaf(aQ[4 * g + i], r2, -m2 * cs[i]);
          if (g < 2) { qa[2 * g] = pk(e[0], e[1]); qa[2 * g + 1] = pk(e[2], e[3]); } else { qb[2 * g - 4] = pk(e[0], e[1]); qb[2 * g - 3] = pk(e[2], e[3]); }
        }
      }
    }
    ST(3);
    {
      CTX();
      if (w == 0) poll(rW, (cu - 16 * (lane + 1)) * 128, OFF_FKV + L * 256 * 128, lane < 2 && pn - 1 - lane >= 0, tlo, thi);
      if (w == 1 && L == 0) {
#pragma unroll 1
        for (int i = 0; i < 4; ++i) poll(rW, lane * 8, OFF_HELLO + i * 512, true, tlo, thi);
        if (b == 0) post(rW, 0, OFF_EPOCH, lane == 0, tlo, thi);
      }
      BAR();
      ST(4);
      const unsigned skv = OFF_KV + L * 4 * MB + j * 64 * 4096;
      v4u ka[9], kb[9], vf[18];
#pragma unroll
      for (int d = 0; d < 9; ++d) {
        const int dd = d == 0 ? 8 : d - 1;
        const bool ok = gtb - 8 + dd >= 0;
        ka[dd] = ld128c(rW, ok ? v16 : (unsigned)(h * 512), skv + (ok ? (gtb - 8 + dd) * 4096 : 0));
      }
#pragma unroll
      for (int d = 0; d < 9; ++d) {
        const int dd = d == 0 ? 8 : d - 1;
        const bool ok = gtb - 8 + dd >= 0;
        const unsigned vo = ok ? v16 : 0x80000000u, so = skv + (ok ? (gtb - 8 + dd) * 4096 : 0);
        vf[2 * dd] = ld128c(rW, vo + 2048, so); vf[2 * dd + 1] = ld128c(rW, vo + 3072, so);
      }
      SB();
      unsigned pm[8];
#pragma unroll
      for (int r = 0; r < 16; r += 2) { const int i0 = (r & 3) + 8 * (r >> 2) + 4 * h; pm[r >> 1] = (i0 <= tok ? 0xffffu : 0u) | (i0 + 1 <= tok ? 0xffff0000u : 0u); }
      const float cnt = gtb < 8 ? (float)(32 * (7 - gtb) + 31 - tok) : 0.f;
      const unsigned so = OFF_O + L * 2 * MB + gtb * 32768 + j * 2048;
      v4u oa = attn_unit<0>(ka, qa, vf, pm, lane, cnt, [&](auto D_) {
        constexpr int dd = D_.value;
        const bool ok = gtb - 8 + dd >= 0;
        kb[dd] = ld128c(rW, (ok ? v16 : (unsigned)(h * 512)) + 1024, skv + (ok ? (gtb - 8 + dd) * 4096 : 0));
      });
      st128c(oa, rW, v16, so);
      SB();
      const unsigned w1s = OFF_W1T + (j * 112 + w * 16) * 1024, w1h = OFF_W1T + (j * 112 + 96 + w * 4) * 1024;
      v4u ob = attn_unit<1>(kb, qb, vf, pm, lane, cnt, [&](auto D_) {
        constexpr int dd = D_.value;
        if constexpr (dd < 4) dmac4(rW, v16, w1s + dd * 4096, LA + w * 16384 + dd * 4096);
        if constexpr (dd == 4) { if (L == 0) dmac4(rW, v16, w1h, LC + w * 4096); }
      });
      st128c(ob, rW, v16 + 1024, so);
      ST(5);
      WAIT_VM0();
      BAR();
      if (w == 0) post(rW, lane * 128, OFF_MBX + ((0 * 2 + L) * 256 + pn * 16) * 128 + j * 8, lane < 16, tlo, thi);
      ST(6);
    }
    {
      CTX();
      const unsigned wr0 = LB + w * 8192 + v16, wr1 = LB + w * 8192 + (v16 ^ 128);
      const unsigned rd = LB + fc * 1024 + (((w * 8 + ft) ^ ((fc & 1) << 3)) * 16);
      v4u wo[16];
#pragma unroll
      for (int i = 0; i < 16; ++i) wo[i] = LDF(rW, v16, OFF_WOT + (ng * 64 + w * 16) * 1024, i);
      if (L == 0) w2_load_range<0, 20>(rW, v16, OFF_W2T + (ng * 224 + w * 56) * 1024);
      SB();
      if (w == 0) poll(rW, lane * 8, OFF_MBX + ((0 * 2 + L) * 256 + cu) * 128, lane < 16, tlo, thi);
      BAR();
      ST(7);
      v4u xo[16];
#pragma unroll
      for (int i = 0; i < 16; ++i) xo[i] = LDF(rW, v16, OFF_O + L * 2 * MB + (pn * 4 + th * 2 + (i >> 3)) * 32768 + w * 8192, i & 7);
      SB();
#pragma unroll
      for (int t = 0; t < 2; ++t) {
        f32x16 a0 = zero16(), a1 = zero16();
#pragma unroll
        for (int s = 0; s < 8; ++s) { a0 = mfma(wo[2 * s], xo[8 * t + s], a0); a1 = mfma(wo[2 * s + 1], xo[8 * t + s], a1); }
#pragma unroll
        for (int g = 0; g < 4; ++g) {
          *(LAS f32x4*)(lds + ((g & 1) ? wr1 : wr0) + t * 32768 + g * 1024) = f32x4{a0[4 * g], a0[4 * g + 1], a0[4 * g + 2], a0[4 * g + 3]};
          *(LAS f32x4*)(lds + ((g & 1) ? wr1 : wr0) + t * 32768 + 4096 + g * 1024) = f32x4{a1[4 * g], a1[4 * g + 1], a1[4 * g + 2], a1[4 * g + 3]};
        }
      }
      BAR();
      ST(8);
#pragma unroll
      for (int t = 0; t < 2; ++t) {
        f32x4 lo = ldsf4(lds + rd + t * 32768), hi = ldsf4(lds + rd + t * 32768 + 512);
#pragma unroll
        for (int q = 1; q < 4; ++q) { lo += ldsf4(lds + rd + t * 32768 + q * 8192); hi += ldsf4(lds + rd + t * 32768 + q * 8192 + 512); }
        v4u r = res[t], o;
        o[0] = pk(lo[0] + bflo(r[0]), lo[1] + bfhi(r[0])); o[1] = pk(lo[2] + bflo(r[1]), lo[3] + bfhi(r[1]));
        o[2] = pk(hi[0] + bflo(r[2]), hi[1] + bfhi(r[2])); o[3] = pk(hi[2] + bflo(r[3]), hi[3] + bfhi(r[3]));
        res[t] = o;
        const int tb = pn * 4 + th * 2 + t;
        st128c(o, rW, (unsigned)((fc >> 1) * 1024 + ((fc & 1) * 32 + ft) * 16), OFF_X1 + L * 2 * MB + tb * 32768 + ng * 4096 + w * 128);
        float S = 0.f, Q = 0.f;
#pragma unroll
        for (int i = 0; i < 4; ++i) { const float a = bflo(o[i]), c = bfhi(o[i]); S += a + c; Q = __builtin_fmaf(a, a, __builtin_fmaf(c, c, Q)); }
        S = xhalf_add(x16_add(x8_add(S))); Q = xhalf_add(x16_add(x8_add(Q)));
        if (lane < 8) st64c(v2u{__builtin_bit_cast(unsigned, S), __builtin_bit_cast(unsigned, Q)}, rW, (unsigned)(lane * 8), OFF_ST + L * 131072 + tb * 2048 + ng * 256 + w * 64);
      }
      ST(9);
      WAIT_VM0();
      BAR();
      if (w == 0) post(rW, lane * 128, OFF_MBX + ((1 * 2 + L) * 256 + pn * 16) * 128 + j * 8, lane < 16, tlo, thi);
      ST(10);
    }
    {
      CTX();
#pragma unroll
      for (int i = 0; i < 8; ++i) dmac(rW, v16, OFF_W1T + (j * 112 + 64 + w * 8 + i) * 1024, LB + (w * 8 + i) * 1024);
      const unsigned vh = LC + ((lane & 15) + 32 * h) * 16;
      if (L == 0) {
        w2_load_range<20, 56>(rW, v16, OFF_W2T + (ng * 224 + w * 56) * 1024);
        SB();
        asm volatile("s_waitcnt vmcnt(36)" ::: "memory");
        BAR();
        f32x16 aC = zero16();
        const unsigned base = w == 0 ? LA + v16 : w == 1 ? LA + 32768 + v16 : w == 2 ? LB + v16 : vh;
        const unsigned s1 = w == 3 ? 256 : 1024, s2 = w == 3 ? 1024 : 2048;
#pragma unroll 4
        for (int s = 0; s < 16; ++s) { aC = mfma(lds128(lds + base + s * s2), ones, aC); aC = mfma(lds128(lds + base + s1 + s * s2), ones, aC); }
        if (tok == 0) {
#pragma unroll
          for (int g = 0; g < 4; ++g) *(LAS f32x4*)(lds + T_CS1 + w * 128 + (8 * g + 4 * h) * 4) = f32x4{aC[4 * g], aC[4 * g + 1], aC[4 * g + 2], aC[4 * g + 3]};
        }
      }
      if (w == 0) poll(rW, lane * 8, OFF_MBX + ((1 * 2 + L) * 256 + cu) * 128, lane < 16, tlo, thi);
      WAIT_VM0();
      BAR();
      ST(11);
      const unsigned sx = OFF_X1 + L * 2 * MB + gtb * 32768;
#pragma unroll
      for (int s = 0; s < WIN; ++s) x[s] = LDF(rW, v16, sx, s);
      SB();
      f32x16 a0 = zero16(), a1 = zero16();
      LAS unsigned char* pa = lds + LA + v16;
      v4u w0 = lds128(pa), w1 = lds128(pa + 32768);
      v2u st[4];
#pragma unroll
      for (int s = 0; s < 32; ++s) {
        if (s + WIN < 32) x[s + WIN] = LDF(rW, v16, sx, s + WIN);
        else if (s + WIN < 36) st[s + WIN - 32] = ld64c(rW, (unsigned)(h * 1024 + tok * 8 + (s + WIN - 32) * 256), OFF_ST + L * 131072 + gtb * 2048);
        v4u n0 = w0, n1 = w1;
        if (s + 1 < 32) { n0 = lds128(pa + (s + 1) * 1024); n1 = lds128(pa + 32768 + (s + 1) * 1024); }
        a0 = mfma(w0, x[s], a0);
        a1 = mfma(w1, x[s], a1);
        w0 = n0; w1 = n1;
        SB();
      }
      float S = 0.f, Q = 0.f;
#pragma unroll
      for (int i = 0; i < 4; ++i) { S += asf(st[i][0]); Q += asf(st[i][1]); }
      S = xhalf_add(S); Q = xhalf_add(Q);
      const float mean = S * (1.f / 512.f), var = fmaxf(Q * (1.f / 512.f) - mean * mean, 0.f);
      const float rs = __builtin_amdgcn_rsqf(var + 1e-5f), mu = mean * rs;
      ST(12);
      if (L == 0) {
        BAR();
        const rsrc_t rQ = mk_rsrc(P->wqkv, 512 * 1536 * 2);
#pragma unroll
        for (int i = 0; i < 8; ++i) { const int s = w * 8 + i;
          dma(rQ, vq, s * 16 * 3072 + 1024 + j * 64, LA + s * 1024); dma(rQ, vq, s * 16 * 3072 + 2048 + j * 64, LA + 32768 + s * 1024); }
      }
      SB();
      const unsigned sh = OFF_H + L * 7 * MB + gtb * 114688 + j * 7168;
      v4u ho;
      auto epi4 = [&](const f32x16& a, int t, int g) {
        const unsigned to = (t * 32 + 8 * g + 4 * h) * 4;
        const f32x4 cs = ldsf4(lds + T_CS1 + to), bb = ldsf4(lds + T_B1 + to);
        float e[4];
#pragma unroll
        for (int i = 0; i < 4; ++i) e[i] = gelu(__builtin_fmaf(a[4 * g + i], rs, __builtin_fmaf(-mu, cs[i], bb[i])));
        ho[2 * (g & 1)] = pk(e[0], e[1]); ho[2 * (g & 1) + 1] = pk(e[2], e[3]);
        if (g & 1) st128c(ho, rW, v16 + ((2 * t + (g >> 1)) & 3) * 1024, sh + ((2 * t + (g >> 1)) >> 2) * 4096);
      };
      f32x16 a2 = zero16(), a3 = zero16();
      LAS unsigned char* pb = lds + LB + v16;
      static_for<0, 12>([&](auto G_) {
        constexpr int G = G_.value, gg = G % 6, s0 = (gg * 16) / 3, s1 = ((gg + 1) * 16) / 3;
#pragma unroll
        for (int s = s0; s < s1; ++s) {
          if constexpr (G < 6) a2 = mfma(lds128(pb + s * 1024), x[s], a2);
          else a3 = mfma(lds128(lds + vh + (s & 1) * 256 + (s >> 1) * 1024), x[s], a3);
        }
        if constexpr (G < 4) epi4(a0, 0, G); else if constexpr (G < 8) epi4(a1, 1, G - 4); else epi4(a2, 2, G - 8);
        __builtin_amdgcn_sched_group_barrier(0x100, 8, 0);
#pragma unroll
        for (int s = s0; s < s1; ++s) { __builtin_amdgcn_sched_group_barrier(0x402, 7, 0); __builtin_amdgcn_sched_group_barrier(0x8, 1, 0); }
        SB();
      });
      epi4(a3, 3, 0); epi4(a3, 3, 1);
      ST(13);
      WAIT_VM0();
      BAR();
      if (w == 0) post(rW, lane * 128, OFF_MBX + ((2 * 2 + L) * 256 + pn * 16) * 128 + j * 8, lane < 16, tlo, thi);
      ST(14);
    }
    {
      CTX();
      if (w == 0) poll(rW, lane * 8, OFF_MBX + ((2 * 2 + L) * 256 + cu) * 128, lane < 16, tlo, thi);
      BAR();
      ST(15);
      const unsigned wr0 = LB + w * 8192 + v16, wr1 = LB + w * 8192 + (v16 ^ 128);
      const unsigned rd = LB + fc * 1024 + (((w * 8 + ft) ^ ((fc & 1) << 3)) * 16);
      constexpr int W2 = 12;
      v4u xh[56];
      const unsigned sh = OFF_H + L * 7 * MB + (pn * 4 + th * 2) * 114688 + w * 28672;
#define LDH(i) LDF(rW, v16, sh + ((i) / 28) * 114688, (i) % 28)
#pragma unroll
      for (int i = 0; i < W2; ++i) xh[i] = LDH(i);
      SB();
#pragma unroll
      for (int t = 0; t < 2; ++t) {
        f32x16 a0, a1;
        static_for<0, 28>([&](auto S_) {
          constexpr int s = S_.value;
          const int i = t * 28 + s;
          if (i + W2 < 56) xh[i + W2] = LDH(i + W2);
          w2_mfma<2 * s, s == 0>(a0, xh[i]); w2_mfma<2 * s + 1, s == 0>(a1, xh[i]);
          SB();
        });
        asm volatile("s_nop 11" : "+v"(a0), "+v"(a1));
#pragma unroll
        for (int g = 0; g < 4; ++g) {
          *(LAS f32x4*)(lds + ((g & 1) ? wr1 : wr0) + t * 32768 + g * 1024) = f32x4{a0[4 * g], a0[4 * g + 1], a0[4 * g + 2], a0[4 * g + 3]};
          *(LAS f32x4*)(lds + ((g & 1) ? wr1 : wr0) + t * 32768 + 4096 + g * 1024) = f32x4{a1[4 * g], a1[4 * g + 1], a1[4 * g + 2], a1[4 * g + 3]};
        }
      }
      BAR();
      ST(16);
      const f32x4 blo = ldsf4(lds + T_B2 + fc * 32), bhi = ldsf4(lds + T_B2 + fc * 32 + 16);
#pragma unroll
      for (int t = 0; t < 2; ++t) {
        f32x4 lo = blo, hi = bhi;
#pragma unroll
        for (int q = 0; q < 4; ++q) { lo += ldsf4(lds + rd + t * 32768 + q * 8192); hi += ldsf4(lds + rd + t * 32768 + q * 8192 + 512); }
        v4u r = res[t], o;
        o[0] = pk(lo[0] + bflo(r[0]), lo[1] + bfhi(r[0])); o[1] = pk(lo[2] + bflo(r[1]), lo[3] + bfhi(r[1]));
        o[2] = pk(hi[0] + bflo(r[2]), hi[1] + bfhi(r[2])); o[3] = pk(hi[2] + bflo(r[3]), hi[3] + bfhi(r[3]));
        res[t] = o;
        const int tb = pn * 4 + th * 2 + t;
        if (L == 0) {
          st128c(o, rW, (unsigned)((fc >> 1) * 1024 + ((fc & 1) * 32 + ft) * 16), OFF_X2 + tb * 32768 + ng * 4096 + w * 128);
          float S = 0.f, Q = 0.f;
#pragma unroll
          for (int i = 0; i < 4; ++i) { const float a = bflo(o[i]), c = bfhi(o[i]); S += a + c; Q = __builtin_fmaf(a, a, __builtin_fmaf(c, c, Q)); }
          S = xhalf_add(x16_add(x8_add(S))); Q = xhalf_add(x16_add(x8_add(Q)));
          if (lane < 8) st64c(v2u{__builtin_bit_cast(unsigned, S), __builtin_bit_cast(unsigned, Q)}, rW, (unsigned)(lane * 8), OFF_ST + 2 * 131072 + tb * 2048 + ng * 256 + w * 64);
        } else {
          __builtin_amdgcn_raw_buffer_store_b128(o, mk_rsrc(P->out, 2048 * 512 * 2), (unsigned)(ft * 1024 + fc * 16), (unsigned)((tb * 32 + w * 8) * 1024 + ng * 128), 0);
        }
      }
      ST(17);
      if (L == 0) {
        WAIT_VM0();
        BAR();
        if (w == 0) post(rW, lane * 128, OFF_MBX + ((3 * 2 + 0) * 256 + pn * 16) * 128 + j * 8, lane < 16, tlo, thi);
        const rsrc_t rQ = mk_rsrc(P->wqkv, 512 * 1536 * 2);
#pragma unroll
        for (int i = 0; i < 8; ++i) { const int s = w * 8 + i; dma(rQ, vq, s * 16 * 3072 + j * 64, LB + s * 1024); }
        ST(18);
      }
    }
    if (L == 1) break;
    {
        CTX();
        if (w == 0) poll(rW, lane * 8, OFF_MBX + ((3 * 2 + 0) * 256 + cu) * 128, lane < 16, tlo, thi);
        WAIT_VM0();
        BAR();
        L = 1; ST(1);
        aK = zero16(); aV = zero16();
        const unsigned sx = OFF_X2 + gtb * 32768;
#pragma unroll
        for (int s = 0; s < WIN; ++s) x[s] = LDF(rW, v16, sx, s);
        SB();
        LAS unsigned char* pa = lds + LA + trb;
        v4u wk = trread(pa), wv = trread(pa + 32768);
        v2u st[4];
#pragma unroll
        for (int s = 0; s < 32; ++s) {
          if (s + WIN < 32) x[s + WIN] = LDF(rW, v16, sx, s + WIN);
          else if (s + WIN < 36) st[s + WIN - 32] = ld64c(rW, (unsigned)(h * 1024 + tok * 8 + (s + WIN - 32) * 256), OFF_ST + 2 * 131072 + gtb * 2048);
          v4u nk = wk, nv = wv;
          if (s + 1 < 32) { nk = trread(pa + (s + 1) * 1024); nv = trread(pa + 32768 + (s + 1) * 1024); }
          aK = mfma(wk, x[s], aK);
          aV = mfma(x[s], wv, aV);
          wk = nk; wv = nv;
          SB();
        }
        float S = 0.f, Q = 0.f;
#pragma unroll
        for (int i = 0; i < 4; ++i) { S += asf(st[i][0]); Q += asf(st[i][1]); }
        S = xhalf_add(S); Q = xhalf_add(Q);
        const float mean = S * (1.f / 512.f), var = fmaxf(Q * (1.f / 512.f) - mean * mean, 0.f);
        rstd = __builtin_amdgcn_rsqf(var + 1e-5f); mur = mean * rstd;
      }
  }
  asm volatile(".Lfused_end:");
}
extern "C" __global__ void __launch_bounds__(256, 1) fused(Params P) {
  asm volatile("s_branch fused_body" ::: "v255", "a255", "s101", "vcc");
}
}

extern "C" void kernel_launch(void* const* d_in, const int* in_sizes, int n_in, void* d_out, int out_size, void* d_ws, size_t ws_size, hipStream_t stream) {
  Params p;
  p.x = (const unsigned char*)d_in[0]; p.wqkv = (const unsigned char*)d_in[1]; p.wo = (const unsigned char*)d_in[2];
  p.w1 = (const unsigned char*)d_in[4]; p.b1 = (const unsigned char*)d_in[5]; p.w2 = (const unsigned char*)d_in[6]; p.b2 = (const unsigned char*)d_in[7];
  p.out = (unsigned char*)d_out; p.ws = (unsigned char*)d_ws;
  (void)hipFuncSetAttribute((const void*)fused, hipFuncAttributeMaxDynamicSharedMemorySize, LDS_BYTES);
  fused<<<256, 256, LDS_BYTES, stream>>>(p);
}
```

```cpp
#include <hip/hip_runtime.h>
#include <stdint.h>
#include <type_traits>

#define WINX 8
#define WINH 8
#define TOUCH 1
#define MAP48 1
namespace {
#define DI __device__ __forceinline__
#define GAS __attribute__((address_space(1)))
#define LAS __attribute__((address_space(3)))
#define CAS __attribute__((address_space(4)))
typedef unsigned v4u __attribute__((ext_vector_type(4)));
typedef unsigned v2u __attribute__((ext_vector_type(2)));
typedef float f32x16 __attribute__((ext_vector_type(16)));
typedef float f32x4 __attribute__((ext_vector_type(4)));
typedef float f32x2 __attribute__((ext_vector_type(2)));
typedef __bf16 bf16x8 __attribute__((ext_vector_type(8)));
typedef __bf16 bf16x4 __attribute__((ext_vector_type(4)));
typedef __bf16 bf16x2 __attribute__((ext_vector_type(2)));
typedef unsigned long long u64;
typedef GAS u64 gu64;
typedef __amdgpu_buffer_rsrc_t rsrc_t;
#define RLX_AGENT __ATOMIC_RELAXED, __HIP_MEMORY_SCOPE_AGENT

constexpr unsigned MB = 1u << 20;
constexpr unsigned OFF_EPOCH = 0, OFF_HELLO = 4096, OFF_MBX = 8192;
constexpr unsigned OFF_FKV = 48 * MB;
constexpr unsigned OFF_FH = OFF_FKV + 262144;
constexpr unsigned OFF_DBG = 512 * 1024;
constexpr unsigned OFF_ST = 640 * 1024;
constexpr unsigned OFF_WOT = 1 * MB;
constexpr unsigned OFF_W1T = OFF_WOT + 512 * 1024;
constexpr unsigned OFF_W2T = OFF_W1T + 1792 * 1024;
constexpr unsigned OFF_X2 = OFF_W2T + 1792 * 1024;
constexpr unsigned OFF_X1 = OFF_X2 + 2 * MB;
constexpr unsigned OFF_O = OFF_X1 + 4 * MB;
constexpr unsigned OFF_KV = OFF_O + 4 * MB;
constexpr unsigned OFF_H = OFF_KV + 8 * MB;
constexpr unsigned WS_BYTES = 49 * MB;
constexpr unsigned LA = 0, LB = 65536, LC = 131072, LT = 147456, LDS_BYTES = 163840;
constexpr unsigned T_AUGQ = LT;
constexpr unsigned T_AUG1 = LT + 2048;
constexpr unsigned T_STAT = LT + 4096;
constexpr unsigned T_B2 = LT + 4608;
constexpr unsigned T_PART = LT + 5120;
constexpr unsigned T_JUNK = LT + 8192;
constexpr unsigned TS = 4128;
constexpr unsigned SPIN_CAP = 1u << 18;

struct Params {
  const unsigned char* x; const unsigned char* wqkv; const unsigned char* wo; const unsigned char* w1;
  const unsigned char* b1; const unsigned char* w2; const unsigned char* b2; unsigned char* out; unsigned char* ws;
};

DI float asf(unsigned u) { return __builtin_bit_cast(float, u); }
DI unsigned asu(float f) { return __builtin_bit_cast(unsigned, f); }
DI bf16x2 asb2(unsigned u) { return __builtin_bit_cast(bf16x2, u); }
DI rsrc_t mk_rsrc(const void* p, unsigned bytes) { return __builtin_amdgcn_make_buffer_rsrc((void*)p, 0, bytes, 0x00020000); }
DI v4u ld128(rsrc_t r, unsigned voff, unsigned soff) { return __builtin_amdgcn_raw_buffer_load_b128(r, voff, soff, 0); }
DI v4u ld128c(rsrc_t r, unsigned voff, unsigned soff) { return __builtin_amdgcn_raw_buffer_load_b128(r, voff, soff, 16); }
DI v2u ld64c(rsrc_t r, unsigned voff, unsigned soff) { return __builtin_amdgcn_raw_buffer_load_b64(r, voff, soff, 16); }
DI void st128c(v4u v, rsrc_t r, unsigned voff, unsigned soff) { __builtin_amdgcn_raw_buffer_store_b128(v, r, voff, soff, 16); }
DI void st64c(v2u v, rsrc_t r, unsigned voff, unsigned soff) { __builtin_amdgcn_raw_buffer_store_b64(v, r, voff, soff, 16); }
#define LDF(r, v16, sbase, i) ld128c(r, (v16) + ((i) & 3) * 1024u, (sbase) + ((i) >> 2) * 4096u)

DI void dma(rsrc_t r, unsigned voff, unsigned soff, unsigned lds) {
  asm volatile("s_mov_b32 m0, %3\n\ts_nop 0\n\tbuffer_load_dwordx4 %0, %1, %2 offen lds" ::"v"(voff), "s"(r), "s"(soff), "s"(lds));
}
DI void dmac(rsrc_t r, unsigned voff, unsigned soff, unsigned lds) {
  asm volatile("s_mov_b32 m0, %3\n\ts_nop 0\n\tbuffer_load_dwordx4 %0, %1, %2 offen sc1 lds" ::"v"(voff), "s"(r), "s"(soff), "s"(lds));
}
DI void dmac4(rsrc_t r, unsigned voff, unsigned soff, unsigned lds) {
  asm volatile("s_mov_b32 m0, %3\n\ts_nop 0\n\tbuffer_load_dwordx4 %0, %1, %2 offen sc1 lds\n\tbuffer_load_dwordx4 %0, %1, %2 offen offset:1024 sc1 lds\n\t"
               "buffer_load_dwordx4 %0, %1, %2 offen offset:2048 sc1 lds\n\tbuffer_load_dwordx4 %0, %1, %2 offen offset:3072 sc1 lds" ::"v"(voff), "s"(r), "s"(soff), "s"(lds));
}
DI void dma_if0(int skip, rsrc_t r, unsigned voff, unsigned soff, unsigned lds) {
  asm volatile("s_cmp_lg_u32 %4, 0\n\ts_cbranch_scc1 1f\n\ts_mov_b32 m0, %3\n\ts_nop 0\n\tbuffer_load_dwordx4 %0, %1, %2 offen lds\n1:" ::"v"(voff), "s"(r), "s"(soff), "s"(lds), "s"(skip) : "scc");
}
template <int OFF> DI v4u ald128(rsrc_t r, unsigned voff, unsigned soff) {
  v4u v; asm volatile("buffer_load_dwordx4 %0, %1, %2, %3 offen offset:%c4 sc1" : "=v"(v) : "v"(voff), "s"(r), "s"(soff), "i"(OFF)); return v;
}
template <int OFF> DI v2u ald64(rsrc_t r, unsigned voff, unsigned soff) {
  v2u v; asm volatile("buffer_load_dwordx2 %0, %1, %2, %3 offen offset:%c4 sc1" : "=v"(v) : "v"(voff), "s"(r), "s"(soff), "i"(OFF)); return v;
}
#define ALDF(r, v16, sbase, i) ald128<((i) & 3) * 1024>(r, v16, (sbase) + ((i) >> 2) * 4096u)
#define WAITV(n, x) asm volatile("s_waitcnt vmcnt(%1)" : "+v"(x) : "n"(n))
DI void touch(rsrc_t r, unsigned voff, unsigned soff, unsigned lds) {
  asm volatile("s_mov_b32 m0, %3\n\ts_nop 0\n\tbuffer_load_dword %0, %1, %2 offen lds" ::"v"(voff), "s"(r), "s"(soff), "s"(lds));
}
template <int S> DI void dma_kv2_if0(int skip, rsrc_t r, unsigned voff, unsigned soff, unsigned lds) {
  unsigned t;
  asm volatile("s_cmp_lg_u32 %5, 0\n\ts_cbranch_scc1 1f\n\t"
               "s_add_u32 m0, %4, %c6\n\ts_add_u32 %0, %3, %c7\n\tbuffer_load_dwordx4 %1, %2, %0 offen lds\n\t"
               "s_add_u32 m0, m0, 31744\n\ts_nop 0\n\tbuffer_load_dwordx4 %1, %2, %0 offen offset:1024 lds\n\t"
               "s_add_u32 m0, m0, 1024\n\ts_add_u32 %0, %0, 49152\n\tbuffer_load_dwordx4 %1, %2, %0 offen offset:1024 lds\n\t"
               "s_sub_u32 m0, m0, 31744\n\ts_nop 0\n\tbuffer_load_dwordx4 %1, %2, %0 offen lds\n1:"
               : "=&s"(t) : "v"(voff), "s"(r), "s"(soff), "s"(lds), "s"(skip), "i"(S * 1024), "i"(S * 49152) : "scc");
}
DI void dmac4j(rsrc_t r, unsigned voff, unsigned soff, unsigned lds) {
  asm volatile("s_mov_b32 m0, %3\n\ts_nop 0\n\tbuffer_load_dwordx4 %0, %1, %2 offen sc1 lds\n\ts_sub_u32 m0, m0, 1024\n\ts_nop 0\n\tbuffer_load_dwordx4 %0, %1, %2 offen offset:1024 sc1 lds\n\t"
               "s_sub_u32 m0, m0, 1024\n\ts_nop 0\n\tbuffer_load_dwordx4 %0, %1, %2 offen offset:2048 sc1 lds\n\ts_sub_u32 m0, m0, 1024\n\ts_nop 0\n\tbuffer_load_dwordx4 %0, %1, %2 offen offset:3072 sc1 lds"
               ::"v"(voff), "s"(r), "s"(soff), "s"(lds) : "scc");
}
template <int OFF> DI void dmac2_if(int go, rsrc_t r, unsigned voff, unsigned soff, unsigned lds) {
  unsigned t;
  asm volatile("s_cmp_eq_u32 %5, 0\n\ts_cbranch_scc1 1f\n\ts_add_u32 m0, %4, %c6\n\ts_add_u32 %0, %3, %c6\n\tbuffer_load_dwordx4 %1, %2, %0 offen sc1 lds\n\t"
               "buffer_load_dwordx4 %1, %2, %0 offen offset:1024 sc1 lds\n1:" : "=&s"(t) : "v"(voff), "s"(r), "s"(soff), "s"(lds), "s"(go), "i"(OFF) : "scc");
}
template <int SO, int LO> DI void dma_o(rsrc_t r, unsigned voff, unsigned soff, unsigned lds) {
  unsigned t;
  asm volatile("s_add_u32 m0, %4, %c6\n\ts_add_u32 %0, %3, %c5\n\tbuffer_load_dwordx4 %1, %2, %0 offen lds" : "=&s"(t) : "v"(voff), "s"(r), "s"(soff), "s"(lds), "i"(SO), "i"(LO) : "scc");
}
template <int OFF> DI void dmac2(rsrc_t r, unsigned voff, unsigned soff, unsigned lds) {
  unsigned t;
  asm volatile("s_add_u32 m0, %4, %c5\n\ts_add_u32 %0, %3, %c5\n\tbuffer_load_dwordx4 %1, %2, %0 offen sc1 lds\n\t"
               "buffer_load_dwordx4 %1, %2, %0 offen offset:1024 sc1 lds" : "=&s"(t) : "v"(voff), "s"(r), "s"(soff), "s"(lds), "i"(OFF) : "scc");
}
#define WAIT_VM0() asm volatile("s_waitcnt vmcnt(0)" ::: "memory")
#define BAR() asm volatile("s_waitcnt lgkmcnt(0)\n\ts_barrier" ::: "memory")
#define SB() __builtin_amdgcn_sched_barrier(0)

DI f32x16 mfma(v4u a, v4u b, f32x16 c) {
  return __builtin_amdgcn_mfma_f32_32x32x16_bf16(__builtin_bit_cast(bf16x8, a), __builtin_bit_cast(bf16x8, b), c, 0, 0, 0);
}
DI v4u trread(LAS unsigned char* p) {
  bf16x4 a = __builtin_amdgcn_ds_read_tr16_b64_v4bf16((LAS bf16x4*)p);
  bf16x4 b = __builtin_amdgcn_ds_read_tr16_b64_v4bf16((LAS bf16x4*)(p + 256));
  return __builtin_bit_cast(v4u, __builtin_shufflevector(a, b, 0, 1, 2, 3, 4, 5, 6, 7));
}
DI v4u lds128(LAS unsigned char* p) { return *(LAS v4u*)p; }
DI f32x4 ldsf4(LAS unsigned char* p) { return *(LAS f32x4*)p; }
DI unsigned pk(float a, float b) { f32x2 v = {a, b}; return __builtin_bit_cast(unsigned, __builtin_convertvector(v, bf16x2)); }
DI float bflo(unsigned u) { return __builtin_bit_cast(float, u << 16); }
DI float bfhi(unsigned u) { return __builtin_bit_cast(float, u & 0xffff0000u); }
DI float xhalf_add(float v) { auto r = __builtin_amdgcn_permlane32_swap(__builtin_bit_cast(unsigned, v), __builtin_bit_cast(unsigned, v), false, false);
  return asf(r[0]) + asf(r[1]); }
DI float xhalf_max(float v) { auto r = __builtin_amdgcn_permlane32_swap(__builtin_bit_cast(unsigned, v), __builtin_bit_cast(unsigned, v), false, false);
  return fmaxf(asf(r[0]), asf(r[1])); }
DI float x16_add(float v) { auto r = __builtin_amdgcn_permlane16_swap(__builtin_bit_cast(unsigned, v), __builtin_bit_cast(unsigned, v), false, false);
  return asf(r[0]) + asf(r[1]); }
DI float x8_add(float v) { return v + __builtin_bit_cast(float, __builtin_amdgcn_update_dpp(0, __builtin_bit_cast(int, v), 0x128, 0xf, 0xf, false)); }
DI f32x16 zero16() { f32x16 z; for (int i = 0; i < 16; ++i) z[i] = 0.f; return z; }

DI void poll(rsrc_t r, unsigned voff, unsigned soff, bool active, unsigned tlo, unsigned thi) {
  for (unsigned spins = 0; spins < SPIN_CAP; ++spins) {
    asm volatile("" ::: "memory");
    const v2u f = ld64c(r, active ? voff : 0x80000000u, soff);
    if (__all(!active || (f[0] == tlo && f[1] == thi))) break;
  }
  asm volatile("" ::: "memory");
}
DI void post(rsrc_t r, unsigned voff, unsigned soff, bool active, unsigned tlo, unsigned thi) { st64c(v2u{tlo, thi}, r, active ? voff : 0x80000000u, soff); }

#define ST(i)

template <int I, int N, class F> DI void static_for(F&& f) { if constexpr (I < N) { f(std::integral_constant<int, I>{}); static_for<I + 1, N>(f); } }
#define IC(n) std::integral_constant<int, n>{}
#define USE(x) asm volatile("" ::"v"(x))
DI float mulf(float a, float b) { float r; asm("v_mul_f32 %0, %1, %2" : "=v"(r) : "v"(a), "v"(b)); return r; }
DI float fmaf_(float a, float b, float c) { float r; asm("v_fma_f32 %0, %1, %2, %3" : "=v"(r) : "v"(a), "v"(b), "v"(c)); return r; }
DI float ex2(float x) { return __builtin_amdgcn_exp2f(x); }
DI float max3(float a, float b, float c) { return fmaxf(fmaxf(a, b), c); }
DI unsigned dup16(unsigned lo16) { return lo16 | (lo16 << 16); }

DI void ln_stats(float S, float Q, int h, float& rstd, float& sd, v4u& xa) {
  S = xhalf_add(S); Q = xhalf_add(Q);
  const float mean = S * (1.f / 512.f), var = fmaxf(__builtin_fmaf(-mean, mean, Q * (1.f / 512.f)), 0.f) + 1e-5f;
  rstd = __builtin_amdgcn_rsqf(var); sd = var * rstd;
  const unsigned hi = pk(-mean, sd);
  const unsigned lo = pk(-mean - bflo(hi), sd - bfhi(hi));
  const unsigned m = (hi & 0xffffu) | (lo << 16), s = (hi >> 16) | (lo & 0xffff0000u);
  xa = h ? v4u{0, 0, 0, 0} : v4u{m, m, s, 0};
}
DI v4u aug_entry(float cs, unsigned b16) {
  const unsigned hi = pk(cs, 0.f) & 0xffffu, lo = pk(cs - bflo(hi), 0.f) & 0xffffu;
  return v4u{dup16(hi), dup16(lo), dup16(b16), 0};
}

DI f32x16 attn_exact(LAS unsigned char* kp, LAS unsigned char* vp, v4u q, int dq) {
  const f32x16 z = zero16();
  float m = -3.0e38f;
#pragma unroll 1
  for (int d = 0; d < 9; ++d) {
    const f32x16 s = mfma(lds128(kp + d * TS), q, z);
    const int hi = d == 8 ? dq : 99, lo = d == 0 ? dq : -99;
#pragma unroll
    for (int r = 0; r < 16; ++r) { const int i = (r & 3) + 8 * (r >> 2); m = (i <= hi && i > lo) ? fmaxf(m, s[r]) : m; }
  }
  m = xhalf_max(m);
  f32x16 o = z;
#pragma unroll 1
  for (int d = 0; d < 9; ++d) {
    const f32x16 s = mfma(lds128(kp + d * TS), q, z);
    const int hi = d == 8 ? dq : 99, lo = d == 0 ? dq : -99;
    float e[16];
#pragma unroll
    for (int r = 0; r < 16; ++r) { const int i = (r & 3) + 8 * (r >> 2); e[r] = (i <= hi && i > lo) ? ex2(s[r] - m) : 0.f; }
    o = mfma(lds128(vp + d * TS), v4u{pk(e[0], e[1]), pk(e[2], e[3]), pk(e[4], e[5]), pk(e[6], e[7])}, o);
    o = mfma(lds128(vp + d * TS + 1040), v4u{pk(e[8], e[9]), pk(e[10], e[11]), pk(e[12], e[13]), pk(e[14], e[15])}, o);
  }
  return o;
}
template <int HD, class Hook>
DI v4u attn_unit(LAS unsigned char* kp, LAS unsigned char* vp, v4u q, const unsigned (&pm)[8], int dq, Hook&& hook) {
  const f32x16 z = zero16();
  f32x16 o, s;
  v4u ph, va, vb, kn;
  {
    const v4u kd = lds128(kp + 8 * TS), kf = lds128(kp), k1 = lds128(kp + TS);
    const v4u vd0 = lds128(vp + 8 * TS), vf0 = lds128(vp), vd1 = lds128(vp + 8 * TS + 1040);
    vb = lds128(vp + 1040); va = lds128(vp + TS); kn = lds128(kp + 2 * TS);
    f32x16 sd = mfma(kd, q, z), sf = mfma(kf, q, z);
    s = mfma(k1, q, z);
    USE(sd); USE(sf); USE(s);
    SB();
    hook(IC(0));
    unsigned p[8];
#pragma unroll
    for (int r = 0; r < 8; r += 2) { const int i0 = (r & 3) + 8 * (r >> 2); p[r >> 1] = pk(ex2((i0 <= dq) ? sd[r] : sf[r]), ex2((i0 + 1 <= dq) ? sd[r + 1] : sf[r + 1])); }
    const v4u pd0 = {p[0] & pm[0], p[1] & pm[1], p[2] & pm[2], p[3] & pm[3]}, pf0 = {p[0] & ~pm[0], p[1] & ~pm[1], p[2] & ~pm[2], p[3] & ~pm[3]};
    USE(pd0); USE(pf0);
    SB();
    o = mfma(vd0, pd0, z);
    USE(o);
    SB();
#pragma unroll
    for (int r = 8; r < 12; r += 2) { const int i0 = (r & 3) + 8 * (r >> 2); p[r >> 1] = pk(ex2((i0 <= dq) ? sd[r] : sf[r]), ex2((i0 + 1 <= dq) ? sd[r + 1] : sf[r + 1])); }
    USE(p[4]); USE(p[5]);
    SB();
    o = mfma(vf0, pf0, o);
    USE(o);
    SB();
#pragma unroll
    for (int r = 12; r < 16; r += 2) { const int i0 = (r & 3) + 8 * (r >> 2); p[r >> 1] = pk(ex2((i0 <= dq) ? sd[r] : sf[r]), ex2((i0 + 1 <= dq) ? sd[r + 1] : sf[r + 1])); }
    const v4u pd1 = {p[4] & pm[4], p[5] & pm[5], p[6] & pm[6], p[7] & pm[7]};
    ph = v4u{p[4] & ~pm[4], p[5] & ~pm[5], p[6] & ~pm[6], p[7] & ~pm[7]};
    USE(pd1); USE(ph);
    SB();
    o = mfma(vd1, pd1, o);
    USE(o);
    SB();
  }
  static_for<1, 8>([&](auto D_) {
    constexpr int d = D_.value;
    float e[16];
    f32x16 sn = s;
#pragma unroll
    for (int i = 0; i < 4; ++i) { e[i] = ex2(s[i]); USE(e[i]); }
    SB();
    o = mfma(vb, ph, o);
    USE(o);
    SB();
#pragma unroll
    for (int i = 4; i < 8; ++i) e[i] = ex2(s[i]);
    const v4u pl = {pk(e[0], e[1]), pk(e[2], e[3]), pk(e[4], e[5]), pk(e[6], e[7])};
    USE(pl);
    SB();
    if constexpr (d + 1 < 8) { sn = mfma(kn, q, z); USE(sn); }
    hook(IC(d));
    SB();
#pragma unroll
    for (int i = 8; i < 12; ++i) { e[i] = ex2(s[i]); USE(e[i]); }
    SB();
    o = mfma(va, pl, o);
    USE(o);
    if constexpr (d + 1 < 8) va = lds128(vp + (d + 1) * TS);
    if constexpr (d + 2 < 8) kn = lds128(kp + (d + 2) * TS);
    vb = lds128(vp + d * TS + 1040);
    SB();
#pragma unroll
    for (int i = 12; i < 16; ++i) e[i] = ex2(s[i]);
    ph = v4u{pk(e[8], e[9]), pk(e[10], e[11]), pk(e[12], e[13]), pk(e[14], e[15])};
    USE(ph);
    s = sn;
    SB();
  });
  o = mfma(vb, ph, o);
  {
    const float l = o[8 - 8 * HD];
    const float a = max3(max3(fabsf(o[8 * HD]), fabsf(o[8 * HD + 1]), fabsf(o[8 * HD + 2])), max3(fabsf(o[8 * HD + 3]), fabsf(o[8 * HD + 4]), fabsf(o[8 * HD + 5])),
                         fabsf(o[8 * HD + 6]) + fabsf(o[8 * HD + 7]));
    const float t = ((o[8 * HD] + o[8 * HD + 1]) + (o[8 * HD + 2] + o[8 * HD + 3])) + (o[8 * HD + 4] + o[8 * HD + 5]);
    const bool safe = l > 1e-18f && l < 1e18f && a < 1e36f && t == t;
    if (__builtin_expect(!__all(safe), 0)) o = attn_exact(kp, vp, q, dq);
  }
  const float inv = __builtin_amdgcn_rcpf(o[8 - 8 * HD]);
  v4u r;
#pragma unroll
  for (int i = 0; i < 4; ++i) r[i] = pk(o[8 * HD + 2 * i] * inv, o[8 * HD + 2 * i + 1] * inv);
  return r;
}

#define CTX() \
  int b = b0, w = w0, lane = lane0; unsigned tlo = tag_lo, thi = tag_hi; const CAS Params* P = P0; \
  asm volatile("" : "+s"(b), "+s"(w), "+s"(tlo), "+s"(thi), "+s"(P)); asm volatile("" : "+v"(lane)); \
  const int tid = w * 64 + lane, pn = MAP48 ? 4 * ((b & 7) >> 1) + ((b >> 3) & 3) : 2 * (b & 7) + (b >> 7), j = MAP48 ? 8 * (b & 1) + (b >> 5) : (b >> 3) & 15, cu = pn * 16 + j, h = lane >> 5, tok = lane & 31; \
  const unsigned v16 = lane * 16; const int th = j & 1, ng = j >> 1, ft = lane & 7, fc = lane >> 3, gtb = pn * 4 + w; \
  const unsigned trb = (8 * h + ((lane & 15) >> 2)) * 64 + ((lane >> 4) & 1) * 32 + (lane & 3) * 8; \
  const unsigned vq = (lane >> 2) * 3072 + (lane & 3) * 16, junk = T_JUNK + w * 1024; \
  (void)tid; (void)cu; (void)tok; (void)v16; (void)th; (void)ng; (void)ft; (void)fc; (void)gtb; (void)trb; (void)vq; (void)junk;

template <int I> DI void w2_load(rsrc_t r, unsigned voff, unsigned soff) {
  asm volatile("buffer_load_dwordx4 a[%c3:%c4], %0, %1, %2 offen offset:%c5 sc1" ::"v"(voff), "s"(r), "s"(soff), "i"(4 * I), "i"(4 * I + 3), "i"((I & 3) * 1024));
}
template <int LO, int HI> DI void w2_load_range(rsrc_t r, unsigned voff, unsigned sbase) {
  if constexpr (LO < HI) { w2_load<LO>(r, voff, sbase + (LO >> 2) * 4096); w2_load_range<LO + 1, HI>(r, voff, sbase); }
}
template <int I> DI void w2_load2_if0(int skip, rsrc_t r, unsigned voff, unsigned sbase) {
  asm volatile("s_cmp_lg_u32 %3, 0\n\ts_cbranch_scc1 1f\n\tbuffer_load_dwordx4 a[%c4:%c5], %0, %1, %2 offen offset:%c8 sc1\n\tbuffer_load_dwordx4 a[%c6:%c7], %0, %1, %2 offen offset:%c9 sc1\n1:"
               ::"v"(voff), "s"(r), "s"(sbase + (I >> 2) * 4096), "s"(skip), "i"(4 * I), "i"(4 * I + 3), "i"(4 * I + 4), "i"(4 * I + 7), "i"((I & 3) * 1024), "i"((I & 3) * 1024 + 1024) : "scc");
}
template <int I> DI void w2_load4_if0(int skip, rsrc_t r, unsigned voff, unsigned sbase) {
  unsigned t;
  asm volatile("s_cmp_lg_u32 %4, 0\n\ts_cbranch_scc1 1f\n\ts_add_u32 %0, %3, %c5\n\tbuffer_load_dwordx4 a[%c6:%c6+3], %1, %2, %0 offen sc1\n\t"
               "buffer_load_dwordx4 a[%c6+4:%c6+7], %1, %2, %0 offen offset:1024 sc1\n\tbuffer_load_dwordx4 a[%c6+8:%c6+11], %1, %2, %0 offen offset:2048 sc1\n\t"
               "buffer_load_dwordx4 a[%c6+12:%c6+15], %1, %2, %0 offen offset:3072 sc1\n1:"
               : "=&s"(t) : "v"(voff), "s"(r), "s"(sbase), "s"(skip), "i"(I * 1024), "i"(4 * I) : "scc");
}
template <int I, bool FIRST> DI void w2_mfma(f32x16& acc, v4u x) {
  if constexpr (FIRST) asm volatile("v_mfma_f32_32x32x16_bf16 %0, a[%c2:%c3], %1, 0" : "=&v"(acc) : "v"(x), "i"(4 * I), "i"(4 * I + 3));
  else asm volatile("v_mfma_f32_32x32x16_bf16 %0, a[%c2:%c3], %1, %0" : "+v"(acc) : "v"(x), "i"(4 * I), "i"(4 * I + 3));
}
DI void red_write(LAS unsigned char* lds, unsigned wr0, unsigned wr1, unsigned off, const f32x16& a, int g) {
  *(LAS f32x4*)(lds + ((g & 1) ? wr1 : wr0) + off + g * 1024) = f32x4{a[4 * g], a[4 * g + 1], a[4 * g + 2], a[4 * g + 3]};
}

extern "C" __global__ void __launch_bounds__(256, 2) fused_body(Params P_) {
  extern __shared__ __attribute__((aligned(16))) unsigned char smem_[];
  LAS unsigned char* lds = (LAS unsigned char*)smem_;
  const int lane0 = threadIdx.x & 63, w0 = __builtin_amdgcn_readfirstlane(threadIdx.x >> 6), b0 = blockIdx.x;
  {
    u64 pc; int len;
    asm volatile("s_getpc_b64 %0\n.Lfused_pc:\n\ts_mov_b32 %1, .Lfused_end-.Lfused_pc" : "=s"(pc), "=s"(len));
    touch(mk_rsrc((const void*)pc, len > 0 ? len : 0), lane0 < 4 ? lane0 * 128u : 0x80000000u, ((b0 >> 3) * 4 + w0) * 512, T_JUNK + w0 * 1024);
  }
  const CAS Params* P0 = (const CAS Params*)__builtin_amdgcn_kernarg_segment_ptr();
  const rsrc_t rW = mk_rsrc(P0->ws, WS_BYTES);
  int L = 0;
  ST(0);
  unsigned tag_lo = 0, tag_hi = 0;
  v4u res[2];
  const v4u ones = {0x3f803f80u, 0x3f803f80u, 0x3f803f80u, 0x3f803f80u};
  constexpr int WIN = WINX, KD = 3;
  v4u x[32], xa;
  f32x16 aK, aV;
  float rstd;
  {
    CTX();
    const unsigned char *px = P->x, *pq = P->wqkv, *pw1 = P->w1, *pw2 = P->w2, *pwo = P->wo, *pb2 = P->b2;
    const int xw = (b >> 3) * 4 + w;
    const int gw = b * 4 + w, rho = lane >> 2, cc = lane & 3, kperm = ((rho >> 2) & 1) * 8 + (rho >> 3) * 4 + (rho & 3);
    unsigned vperm = kperm * 1024 + cc * 16, vnat1 = rho * 3584 + cc * 16;
    unsigned pv1 = gw < 768 ? vperm : (rho + 16 * (cc >> 1)) * 3584 + (cc & 1) * 16, pv3 = gw < 512 ? vnat1 : vperm;
    unsigned ps0 = (gw >> 4) * 16384 + (gw & 15) * 64;
    unsigned ps1 = gw < 768 ? ps0 + 64 * 16384 : ((gw - 768) >> 4) * 32 * 3584 + (gw & 15) * 224 + 192;
    unsigned ps2 = (gw >> 5) * 16 * 3584 + ((gw & 31) >> 1) * 224 + (gw & 1) * 64;
    unsigned ps3 = gw < 512 ? (gw >> 4) * 16 * 3584 + (gw & 15) * 224 + 128 : ps0 - 32 * 16384;
    unsigned sx = gtb * 32768, stg = LB + w * 8192, kvs = w * 2 * 49152 + 1024 + j * 64, kvl = LA + w * 2048, qs = w * 8 * 49152 + j * 64;
    unsigned vxs = (lane >> 4) * 1024 + (((lane & 15) ^ (lane >> 4)) * 16), vqq = vq;
    asm volatile("" : "+s"(ps0), "+s"(ps1), "+s"(ps2), "+s"(ps3), "+s"(sx), "+s"(stg), "+s"(kvs), "+s"(kvl), "+s"(qs));
    asm volatile("" : "+v"(pv1), "+v"(pv3), "+v"(vperm), "+v"(vnat1), "+v"(vxs), "+v"(vqq));
    SB();
    const rsrc_t rX = mk_rsrc(px, 2048 * 512 * 2), rQ = mk_rsrc(pq, 512 * 1536 * 2);
    const unsigned vx4[4] = {vxs, vxs ^ 64, vxs ^ 128, vxs ^ 192};
    auto xdma = [&](auto Q_, auto I_) { constexpr int q = Q_.value, i = I_.value; dma_o<i * 4096 + q * 256, (q & 1) * 32768 + i * 1024>(rX, vx4[i & 3], sx, stg); };
    static_for<0, 8>([&](auto I_) { xdma(IC(0), I_); });
    dma_kv2_if0<0>(0, rQ, vqq, kvs, kvl);
    touch(rX, lane < (MAP48 ? 32 : 16) ? lane * 128u : 0x80000000u, MAP48 ? ((b & 7) >> 1) * 524288 + xw * 4096 : (b & 7) * 262144 + xw * 2048, junk);
    touch(rQ, MAP48 ? (lane < 48 ? (lane >> 2) * 1024u + (lane & 3) * 128u : 0x80000000u) : (lane >> 4) * 3072u + (lane & 15) * 128u, xw * 4 * 3072 + (MAP48 ? (b & 1) * 512 : 1024), junk);
    v2u e = ald64<0>(rW, 0, OFF_EPOCH);
    unsigned bt;
    asm volatile("buffer_load_ushort %0, %1, %2, %3 offen" : "=v"(bt) : "v"(lane * 2), "s"(mk_rsrc(pb2, 1024)), "s"(ng * 128));
    const rsrc_t rP0 = mk_rsrc(pw2, 1792 * 1024), rP1 = mk_rsrc(gw < 768 ? pw2 : pw1, 1792 * 1024), rP2 = mk_rsrc(pw1, 1792 * 1024), rP3 = mk_rsrc(gw < 512 ? pw1 : pwo, gw < 512 ? 1792 * 1024 : 512 * 1024);
    SB();
    ST(1);
    float s0 = 0.f, s1 = 0.f, q0 = 0.f, q1 = 0.f;
    const bf16x2 one2 = asb2(0x3f803f80u);
    f32x16 aC = zero16();
    aK = zero16(); aV = zero16();
    LAS unsigned char* pa = lds + LA + trb;
    LAS unsigned char* po = pa + w * 2048;
    const unsigned xr = stg + tok * 256 + ((h ^ (tok & 15)) * 16);
    const unsigned onesk = (lane & 16) ? 0u : 0x3f803f80u, onesv = onesk ^ 0x3f803f80u;
    const v4u selk = {onesk, onesk, onesk, onesk}, selv = {onesv, onesv, onesv, onesv};
    static_for<0, 4>([&](auto C_) {
      constexpr int c = C_.value;
      asm volatile("s_waitcnt vmcnt(%0)" ::"n"(c == 0 ? 6 + 2 * TOUCH : 6) : "memory");
#pragma unroll
      for (int s = 0; s < 8; ++s) x[8 * c + s] = lds128(lds + (xr ^ (s * 32)) + (c & 1) * 32768);
      SB();
      static_for<0, 8>([&](auto I_) {
        constexpr int i = I_.value, s = 8 * c + i;
        if constexpr (c < 3) xdma(IC(c + 1), I_); else dma_o<i * 49152, i * 1024>(rQ, vqq, qs, stg);
#pragma unroll
        for (int k = 0; k < 4; k += 2) {
          const bf16x2 u = asb2(x[s][k]), v = asb2(x[s][k + 1]);
          s0 = __builtin_amdgcn_fdot2_f32_bf16(u, one2, s0, false); s1 = __builtin_amdgcn_fdot2_f32_bf16(v, one2, s1, false);
          q0 = __builtin_amdgcn_fdot2_f32_bf16(u, u, q0, false); q1 = __builtin_amdgcn_fdot2_f32_bf16(v, v, q1, false);
        }
        USE(s0); USE(s1); USE(q0); USE(q1);
        SB();
      });
      asm volatile("s_waitcnt vmcnt(%0)" ::"n"(c == 0 ? 10 + 2 * TOUCH : 10) : "memory");
      const v4u ck0 = trread(po + c * 8192), ck1 = trread(po + c * 8192 + 1024), cv0 = trread(po + 32768 + c * 8192), cv1 = trread(po + 32768 + c * 8192 + 1024);
      asm volatile("s_barrier" ::: "memory");
      v4u wk[8], wv[8];
#pragma unroll
      for (int i = 0; i < 2; ++i) { wv[i] = trread(pa + 32768 + (8 * c + i) * 1024); wk[i] = trread(pa + (8 * c + i) * 1024); }
      SB();
      aC = mfma(selk, ck0, aC); aC = mfma(selk, ck1, aC); aC = mfma(selv, cv0, aC); aC = mfma(selv, cv1, aC);
      USE(aC);
      wv[2] = trread(pa + 32768 + (8 * c + 2) * 1024); wk[2] = trread(pa + (8 * c + 2) * 1024);
      SB();
      static_for<0, 8>([&](auto I_) {
        constexpr int i = I_.value, s = 8 * c + i;
        aK = mfma(wk[i], x[s], aK);
        USE(aK);
        if constexpr (c < 3 && i < 4) dma_o<(8 * (c + 1) + (i >> 1)) * 49152 + (i & 1) * 1024, (8 * (c + 1) + (i >> 1)) * 1024 + (i & 1) * 32768>(rQ, vqq, kvs, kvl);
        if constexpr (c == 0 && i == 4) dma(rP0, vperm, ps0, LC + w * 4096);
        if constexpr (c == 0 && i == 6) dma(rP1, pv1, ps1, LC + w * 4096 + 1024);
        if constexpr (c == 1 && i == 4) dma(rP2, vnat1, ps2, LC + w * 4096 + 2048);
        if constexpr (c == 1 && i == 6) dma(rP3, pv3, ps3, LC + w * 4096 + 3072);
        if constexpr (c == 2 && (i == 4 || i == 6)) res[(i >> 1) & 1] = ald128<0>(rX, (unsigned)(ft * 1024 + fc * 16), (unsigned)(((pn * 4 + th * 2 + ((i >> 1) & 1)) * 32 + w * 8) * 1024 + ng * 128));
        SB();
        aV = mfma(x[s], wv[i], aV);
        USE(aV);
        if constexpr (i + KD < 8) { wv[i + KD] = trread(pa + 32768 + (s + KD) * 1024); wk[i + KD] = trread(pa + (s + KD) * 1024); }
        if constexpr (c == 3 && i == 2) *(LAS float*)(lds + T_PART + w * 256 + lane * 4) = h ? aC[8] : aC[0];
        SB();
      });
    });
    asm volatile("s_waitcnt vmcnt(8)" : "+v"(e), "+v"(res[0]), "+v"(res[1]), "+v"(bt)::"memory");
    float sd;
    ln_stats(s0 + s1, q0 + q1, h, rstd, sd, xa);
    const u64 tag = ((u64)(unsigned)__builtin_amdgcn_readfirstlane(e[1]) << 32 | (unsigned)__builtin_amdgcn_readfirstlane(e[0])) + 1;
    tag_lo = (unsigned)tag; tag_hi = (unsigned)(tag >> 32);
    *(LAS float*)(lds + T_B2 + lane * 4) = bflo(bt);
    BAR();
    LAS float* pp = (LAS float*)(lds + T_PART + lane * 4);
    *(LAS v4u*)(lds + T_AUGQ + lane * 16) = aug_entry((pp[0] + pp[64]) + (pp[128] + pp[192]), 0);
  }
#pragma unroll 1
  for (;;) {
    v4u qa, qb;
    {
      ST(2);
      CTX();
      const unsigned skv = OFF_KV + L * 4 * MB + (j * 64 + gtb) * 4096;
      LAS unsigned char* pb = lds + LB + trb;
      constexpr int QD = 5, QFLAG = 20;
      v4u wq[32];
      {
        *(LAS float*)(lds + T_STAT + w * 128 + tok * 4) = rstd;
        const v4u gk = lds128(lds + T_AUGQ + tok * 16), gv = lds128(lds + T_AUGQ + 512 + tok * 16);
        f32x4 rs[4];
#pragma unroll
        for (int g = 0; g < 4; ++g) rs[g] = ldsf4(lds + T_STAT + w * 128 + (8 * g + 4 * h) * 4);
        aK = mfma(gk, xa, aK);
        aV = mfma(xa, gv, aV);
        USE(aK); USE(aV);
        SB();
        v4u ka, kb, v0, v1;
#pragma unroll
        for (int i = 0; i < 4; ++i) { ka[i] = pk(aK[2 * i] * rstd, aK[2 * i + 1] * rstd); kb[i] = pk(aK[8 + 2 * i] * rstd, aK[9 + 2 * i] * rstd); }
        st128c(ka, rW, v16, skv); st128c(kb, rW, v16 + 1024, skv);
        SB();
#pragma unroll
        for (int i = 0; i < 4; ++i) {
          v0[i] = pk(aV[2 * i] * rs[i >> 1][(2 * i) & 3], aV[2 * i + 1] * rs[i >> 1][(2 * i + 1) & 3]);
          v1[i] = pk(aV[8 + 2 * i] * rs[2 + (i >> 1)][(2 * i) & 3], aV[9 + 2 * i] * rs[2 + (i >> 1)][(2 * i + 1) & 3]);
        }
        st128c(v0, rW, v16 + 2048, skv); st128c(v1, rW, v16 + 3072, skv);
        asm volatile("s_waitcnt vmcnt(4)\n\ts_barrier" ::: "memory");
        if (L == 0)
        {
          const int gw = b * 4 + w;
          LAS unsigned char* sc = lds + LC + w * 4096 + trb;
          v4u t0 = trread(sc), t1 = trread(sc + 1024), t2 = trread(sc + 2048), t3 = trread(sc + 3072);
          const unsigned d0 = ((((gw & 15) >> 1) * 112 + (gw >> 4)) * 2 + (gw & 1)) * 1024;
          st128c(t0, rW, v16, OFF_W2T + d0);
          st128c(t1, rW, v16, gw < 768 ? OFF_W2T + d0 + 128 * 1024 : OFF_W1T + ((gw & 15) * 112 + 96 + ((gw - 768) >> 4)) * 1024);
          st128c(t2, rW, v16, OFF_W1T + (((gw & 31) >> 1) * 112 + (gw & 1) * 32 + (gw >> 5)) * 1024);
          st128c(t3, rW, v16, gw < 512 ? OFF_W1T + ((gw & 15) * 112 + 64 + (gw >> 4)) * 1024 : OFF_WOT + ((((gw & 15) >> 1) * 32 + ((gw - 512) >> 4)) * 2 + (gw & 1)) * 1024);
        }
#pragma unroll
        for (int s = 0; s < QD; ++s) wq[s] = trread(pb + s * 1024);
        LAS unsigned char* own = lds + LB + (8 + w) * TS + v16;
        *(LAS v4u*)(own) = v0; *(LAS v4u*)(own + 1040) = v1; *(LAS v4u*)(own + 2080) = ka; *(LAS v4u*)(own + 3104) = kb;
      }
      SB();
      f32x16 aQ = zero16();
      v4u gq;
#pragma unroll
      for (int s = 0; s < 32; ++s) {
        aQ = mfma(wq[s], x[s], aQ);
        USE(aQ);
        if (s + QD < 32) wq[s + QD] = trread(pb + (s + QD) * 1024);
        SB();
        if (s == QFLAG) {
          WAIT_VM0();
          post(rW, 0, OFF_FKV + ((L * 256 + cu) * 4 + w) * 128, lane == 0, tlo, thi);
          dmac4j(rW, v16, skv, junk);
          SB();
        }
      }
      if (L == 0) {
        f32x16 aC = zero16();
#pragma unroll
        for (int i = 0; i < 8; ++i) aC = mfma(ones, trread(pb + w * 8192 + i * 1024), aC);
        *(LAS float*)(lds + T_AUGQ + 1536 + w * 128 + tok * 4) = aC[0];
        BAR();
        if (w == 1) post(rW, 0, OFF_HELLO + b * 8, lane == 0, tlo, thi);
        LAS float* pp = (LAS float*)(lds + T_AUGQ + 1536 + tok * 4);
        gq = aug_entry((pp[0] + pp[32]) + (pp[64] + pp[96]), 0);
        if (w == 0) *(LAS v4u*)(lds + T_AUGQ + 1024 + tok * 16) = gq;
      } else {
        gq = lds128(lds + T_AUGQ + 1024 + tok * 16);
        BAR();
      }
      aQ = mfma(gq, xa, aQ);
      const float r2 = rstd * (0.25f * 1.4426950408889634f);
#pragma unroll
      for (int i = 0; i < 4; ++i) { qa[i] = pk(aQ[2 * i] * r2, aQ[2 * i + 1] * r2); qb[i] = pk(aQ[8 + 2 * i] * r2, aQ[9 + 2 * i] * r2); }
      USE(qa); USE(qb);
    }
    ST(3);
    {
      CTX();
      if (w == 3 && lane < 24) *(LAS v4u*)(lds + LB + (lane >> 1) * TS + 1024 + (lane & 1) * 1040) = (pn * 4 - 8 + (lane >> 1) >= 0) ? ones : v4u{0, 0, 0, 0};
      unsigned pm[8];
      const int dq = tok - 4 * h;
#pragma unroll
      for (int r = 0; r < 16; r += 2) { const int i0 = (r & 3) + 8 * (r >> 2); pm[r >> 1] = (i0 <= dq ? 0xffffu : 0u) | (i0 + 1 <= dq ? 0xffff0000u : 0u); asm volatile("" : "+v"(pm[r >> 1])); }
      const int src = pn - 2 + (w >> 1);
      const bool ok = src >= 0;
      const unsigned skv = OFF_KV + L * 4 * MB + j * 64 * 4096 + (ok ? (pn * 4 - 8 + 2 * w) * 4096 : 0), st = ok ? 4096 : 0;
      const unsigned vk = ok ? v16 : (unsigned)(h * 512), vv = ok ? v16 : 0x80000000u, dst = LB + 2 * w * TS;
      poll(rW, lane * 128, OFF_FKV + ((L * 256 + (ok ? src : 0) * 16 + j) * 4 + (ok ? 2 * (w & 1) : 0)) * 128, lane < 2, tlo, thi);
      ST(4);
      dmac(rW, vk, skv, dst + 2080); dmac(rW, vk, skv + st, dst + TS + 2080);
      dmac(rW, vv, skv + 2048, dst); dmac(rW, vv, skv + st + 2048, dst + TS);
      dmac(rW, vv, skv + 3072, dst + 1040); dmac(rW, vv, skv + st + 3072, dst + TS + 1040);
      dmac(rW, vk, skv + 1024, dst + 3104); dmac(rW, vk, skv + st + 1024, dst + TS + 3104);
      const unsigned so = OFF_O + L * 2 * MB + gtb * 32768 + j * 2048;
      LAS unsigned char* tb = lds + LB + w * TS;
      unsigned w1s = OFF_W1T + (j * 112 + w * 16) * 1024, w1l = LA + w * 16384, w2s = OFF_W2T + (ng * 224 + w * 56) * 1024;
      asm volatile("" : "+s"(w1s), "+s"(w1l), "+s"(w2s));
      asm volatile("s_waitcnt vmcnt(2) lgkmcnt(0)\n\ts_barrier" ::: "memory");
      ST(19);
      v4u oa = attn_unit<0>(tb + 2080 + v16, tb + ((lane & 16) ? 1024u : v16), qa, pm, dq,
        [&](auto D_) { constexpr int d = D_.value;
          if constexpr (d == 1) {
            if (L == 0) {
              poll(rW, lane * 8, OFF_HELLO + w * 512, true, tlo, thi);
            }
            asm volatile("s_waitcnt vmcnt(0)\n\ts_barrier" ::: "memory");
            dmac2<0>(rW, v16, w1s, w1l);
          }
          if constexpr (d >= 1) dmac2<d * 2048>(rW, v16, w1s, w1l); });
      st128c(oa, rW, v16, so);
      ST(20);
      if (L == 0) {
        if (b == 0 && w == 0) post(rW, 0, OFF_EPOCH, lane == 0, tlo, thi);
        dmac4(rW, v16, OFF_W1T + (j * 112 + 96 + w * 4) * 1024, LC + w * 4096);
      }
      SB();
      v4u ob = attn_unit<1>(tb + 3104 + v16, tb + ((lane & 16) ? v16 : 1024u), qb, pm, dq,
        [&](auto D_) { constexpr int d = D_.value; if constexpr (d < 6) w2_load4_if0<4 * d>(L, rW, v16, w2s); });
      st128c(ob, rW, v16 + 1024, so);
      ST(5);
      WAIT_VM0();
      dmac(rW, v16, so, junk); dmac(rW, v16, so + 1024, junk);
      BAR();
      if (w == 0) post(rW, lane * 128, OFF_MBX + ((0 * 2 + L) * 256 + pn * 16) * 128 + j * 8, lane < 16, tlo, thi);
      ST(6);
    }
#define FINALIZE(FFN, XOFF, STOFF) \
      { \
        f32x4 lo[2][4], hi[2][4]; \
        _Pragma("unroll") for (int t = 0; t < 2; ++t) _Pragma("unroll") for (int q = 0; q < 4; ++q) { \
          lo[t][q] = ldsf4(lds + rd + t * 32768 + q * 8192); hi[t][q] = ldsf4(lds + rd + t * 32768 + q * 8192 + 512); } \
        SB(); \
        float S[2], Q[2]; \
        const unsigned vo = (fc >> 1) * 1024 + ((fc & 1) * 32 + ft) * 16, xo_ = (XOFF) + (pn * 4 + th * 2) * 32768 + ng * 4096 + w * 128; \
        _Pragma("unroll") for (int t = 0; t < 2; ++t) { \
          const v4u r = res[t]; \
          f32x4 l4 = f32x4{bflo(r[0]), bfhi(r[0]), bflo(r[1]), bfhi(r[1])}, h4 = f32x4{bflo(r[2]), bfhi(r[2]), bflo(r[3]), bfhi(r[3])}; \
          if (FFN) { l4 += blo; h4 += bhi; } \
          l4 += (lo[t][0] + lo[t][1]) + (lo[t][2] + lo[t][3]); h4 += (hi[t][0] + hi[t][1]) + (hi[t][2] + hi[t][3]); \
          res[t] = v4u{pk(l4[0], l4[1]), pk(l4[2], l4[3]), pk(h4[0], h4[1]), pk(h4[2], h4[3])}; \
          if (!(FFN)) st128c(res[t], rW, vo, xo_ + t * 32768); \
        } \
        if (FFN) { \
          ST(17); \
          const unsigned so_ = ((pn * 4 + th * 2) * 32 + w * 8) * 1024 + ng * 128; \
          asm volatile("s_cmp_eq_u32 %6, 0\n\ts_cbranch_scc1 1f\n\tbuffer_store_dwordx4 %0, %2, %3, %4 offen\n\tbuffer_store_dwordx4 %1, %2, %3, %5 offen\n\ts_endpgm\n1:" \
                       ::"v"(res[0]), "v"(res[1]), "v"((unsigned)(ft * 1024 + fc * 16)), "s"(rOut), "s"(so_), "s"(so_ + 32768), "s"(L) : "scc", "memory"); \
          st128c(res[0], rW, vo, xo_); st128c(res[1], rW, vo, xo_ + 32768); \
        } \
        const bf16x2 one2 = asb2(0x3f803f80u); \
        _Pragma("unroll") for (int t = 0; t < 2; ++t) { S[t] = 0.f; Q[t] = 0.f; \
          _Pragma("unroll") for (int i = 0; i < 4; ++i) { const bf16x2 u = asb2(res[t][i]); S[t] = __builtin_amdgcn_fdot2_f32_bf16(u, one2, S[t], false); Q[t] = __builtin_amdgcn_fdot2_f32_bf16(u, u, Q[t], false); } } \
        _Pragma("unroll") for (int t = 0; t < 2; ++t) { S[t] = x8_add(S[t]); Q[t] = x8_add(Q[t]); } \
          \
        auto rS = __builtin_amdgcn_permlane32_swap(asu(S[0]), asu(S[1]), false, false); auto rQ_ = __builtin_amdgcn_permlane32_swap(asu(Q[0]), asu(Q[1]), false, false); \
        const float Sx = x16_add(asf(rS[0]) + asf(rS[1])), Qx = x16_add(asf(rQ_[0]) + asf(rQ_[1])); \
        st64c(v2u{asu(Sx), asu(Qx)}, rW, (lane & 24) ? 0x80000000u : (unsigned)(h * 2048 + ft * 8), (STOFF) + (pn * 4 + th * 2) * 2048 + ng * 256 + w * 64); \
        WAIT_VM0(); \
        dmac(rW, vo, xo_, junk); dmac(rW, vo, xo_ + 32768, junk); \
      }
    {
      CTX();
      const unsigned wr0 = LB + w * 8192 + v16, wr1 = LB + w * 8192 + (v16 ^ 128);
      const unsigned rd = LB + fc * 1024 + (((w * 8 + ft) ^ ((fc & 1) << 3)) * 16);
      v4u wo[16], xo[16];
      static_for<0, 16>([&](auto I_) { constexpr int i = I_.value; wo[i] = ALDF(rW, v16, OFF_WOT + (ng * 64 + w * 16) * 1024, i); });
      if (L == 0) {
        const unsigned onesk = (lane & 16) ? 0u : 0x3f803f80u, onesv = onesk ^ 0x3f803f80u;
        const v4u selk = {onesk, onesk, onesk, onesk}, selv = {onesv, onesv, onesv, onesv};
        f32x16 aC = zero16();
#pragma unroll
        for (int i = 0; i < 16; ++i) aC = mfma(selk, lds128(lds + LA + w * 16384 + v16 + i * 1024), aC);
#pragma unroll
        for (int i = 0; i < 4; ++i) aC = mfma(selv, lds128(lds + LC + w * 4096 + v16 + i * 1024), aC);
        *(LAS float*)(lds + T_PART + w * 256 + lane * 4) = h ? aC[8] : aC[0];
      }
      unsigned sxo = OFF_O + L * 2 * MB + (pn * 4 + th * 2) * 32768 + w * 8192;
      asm volatile("" : "+s"(sxo));
      if (w == 0) poll(rW, lane * 8, OFF_MBX + ((0 * 2 + L) * 256 + cu) * 128, lane < 16, tlo, thi);
      BAR();
      ST(7);
      constexpr int WO_W = 6;
#define LDO(i) ALDF(rW, v16, sxo + ((i) >> 3) * 32768, (i) & 7)
      static_for<0, WO_W>([&](auto I_) { constexpr int i = I_.value; xo[i] = LDO(i); });
      SB();
      f32x16 a0 = zero16(), a1 = zero16(), a2 = zero16(), a3 = zero16();
      static_for<0, 16>([&](auto I_) {
        constexpr int i = I_.value, s = i & 7;
        asm volatile("s_waitcnt vmcnt(%3)" : "+v"(xo[i]), "+v"(wo[2 * s]), "+v"(wo[2 * s + 1]) : "n"(i + WO_W <= 16 ? WO_W - 1 : 15 - i));
        if constexpr (i < 8) { a0 = mfma(wo[2 * s], xo[i], a0); USE(a0); } else { a2 = mfma(wo[2 * s], xo[i], a2); USE(a2); }
        if constexpr (i + WO_W < 16) xo[i + WO_W] = LDO(i + WO_W);
        SB();
        if constexpr (i < 8) { a1 = mfma(wo[2 * s + 1], xo[i], a1); USE(a1); } else { a3 = mfma(wo[2 * s + 1], xo[i], a3); USE(a3); }
        if constexpr (i >= 10 && i < 14) { red_write(lds, wr0, wr1, 0, a0, i - 10); red_write(lds, wr0, wr1, 4096, a1, i - 10); }
        SB();
      });
#pragma unroll
      for (int g = 0; g < 4; ++g) red_write(lds, wr0, wr1, 32768, a2, g);
#pragma unroll
      for (int g = 0; g < 4; ++g) red_write(lds, wr0, wr1, 32768 + 4096, a3, g);
      BAR();
      ST(8);
      const f32x4 blo = {0, 0, 0, 0}, bhi = blo; const rsrc_t rOut = rW;
      FINALIZE(false, OFF_X1 + L * 2 * MB, OFF_ST + L * 131072)
      ST(9);
      BAR();
      if (w == 0) post(rW, lane * 128, OFF_MBX + ((1 * 2 + L) * 256 + pn * 16) * 128 + j * 8, lane < 16, tlo, thi);
      ST(10);
    }
    {
      CTX();
      dmac4(rW, v16, OFF_W1T + (j * 112 + 64 + w * 8) * 1024, LB + w * 8192); dmac4(rW, v16, OFF_W1T + (j * 112 + 68 + w * 8) * 1024, LB + w * 8192 + 4096);
      const unsigned vh = LC + ((lane & 15) + 32 * h) * 16;
      unsigned w2s = OFF_W2T + (ng * 224 + w * 56) * 1024;
      unsigned bt0 = 0, bt1 = 0;
      if (L == 0) {
        const rsrc_t rB1 = mk_rsrc(P->b1, 3584);
        asm volatile("buffer_load_ushort %0, %2, %4, %5 offen\n\tbuffer_load_ushort %1, %3, %4, %5 offen offset:128" : "=&v"(bt0), "=&v"(bt1)
                     : "v"(lane * 2), "v"((h ? 32 + (tok & 15) : tok) * 2), "s"(rB1), "s"(j * 224));
        w2_load_range<24, 32>(rW, v16, w2s);
        asm volatile("s_waitcnt vmcnt(8)" : "+v"(bt0), "+v"(bt1)::"memory");
        f32x16 aC = zero16();
#pragma unroll
        for (int i = 0; i < 8; ++i) aC = mfma(ones, lds128(lds + LB + w * 8192 + v16 + i * 1024), aC);
        *(LAS float*)(lds + T_PART + 1024 + w * 128 + tok * 4) = aC[0];
      }
      const rsrc_t rQ = mk_rsrc(P->wqkv, 512 * 1536 * 2);
      unsigned sh = OFF_H + L * 7 * MB + gtb * 114688 + j * 7168, kvs = w * 8 * 49152 + 1024 + j * 64, kvl = LA + w * 8192, sx = OFF_X1 + L * 2 * MB + gtb * 32768;
      asm volatile("" : "+s"(sh), "+s"(kvs), "+s"(kvl), "+s"(sx), "+s"(w2s) : "s"(rQ));
      if (w == 0) poll(rW, lane * 8, OFF_MBX + ((1 * 2 + L) * 256 + cu) * 128, lane < 16, tlo, thi);
      WAIT_VM0();
      BAR();
      ST(11);
      v4u x[32], xa;
      v2u st[4];
      static_for<0, 4>([&](auto S_) { constexpr int s = S_.value; x[s] = ALDF(rW, v16, sx, s); });
      static_for<0, 4>([&](auto I_) { constexpr int i = I_.value; st[i] = ald64<i * 256>(rW, (unsigned)(h * 1024 + tok * 8), OFF_ST + L * 131072 + gtb * 2048); });
      static_for<4, WIN>([&](auto S_) { constexpr int s = S_.value; x[s] = ALDF(rW, v16, sx, s); });
      if (L == 0) {
        LAS float* p1 = (LAS float*)(lds + T_PART + h * 512 + tok * 4);
        LAS float* p2 = (LAS float*)(lds + T_PART + (h ? 128 + (tok & 15) * 4 : 1024 + tok * 4));
        const int st2 = h ? 64 : 32;
        float c2 = (p2[0] + p2[st2]) + (p2[2 * st2] + p2[3 * st2]);
        if (h) c2 += (p2[16] + p2[80]) + (p2[144] + p2[208]);
        *(LAS v4u*)(lds + T_AUG1 + lane * 16) = aug_entry(p1[0] + p1[64], bt0);
        *(LAS v4u*)(lds + T_AUG1 + 1024 + lane * 16) = aug_entry(c2, bt1);
      }
      f32x16 a0 = zero16(), a1 = zero16();
      LAS unsigned char* pa = lds + LA + v16;
      LAS unsigned char* pb = lds + LB + v16;
      v4u f0[32], f1[32], f2[64], g1[4];
#pragma unroll
      for (int s = 0; s < KD; ++s) { f1[s] = lds128(pa + 32768 + s * 1024); f0[s] = lds128(pa + s * 1024); }
      SB();
      float rs, sd;
      static_for<0, 32>([&](auto S_) {
        constexpr int s = S_.value;
        WAITV((s + WIN <= 32 ? WIN - 1 : 31 - s) + (s < 4 ? 4 : 0), x[s]);
        a0 = mfma(f0[s], x[s], a0);
        USE(a0);
        if constexpr (s + WIN < 32) x[s + WIN] = ALDF(rW, v16, sx, s + WIN);
        a1 = mfma(f1[s], x[s], a1);
        USE(a1);
        if constexpr (s + KD < 32) { f1[s + KD] = lds128(pa + 32768 + (s + KD) * 1024); f0[s + KD] = lds128(pa + (s + KD) * 1024); }
        if constexpr (s == 29) { f2[1] = lds128(pb + 1024); f2[0] = lds128(pb); }
        if constexpr (s == 30) { f2[3] = lds128(pb + 3072); f2[2] = lds128(pb + 2048); }
        if constexpr (s == 31) { g1[3] = lds128(lds + T_AUG1 + 1536 + tok * 16); g1[2] = lds128(lds + T_AUG1 + 1024 + tok * 16); }
        if constexpr (s == 6) {
          asm volatile("" : "+v"(st[0]), "+v"(st[1]), "+v"(st[2]), "+v"(st[3]));
          ln_stats((asf(st[0][0]) + asf(st[1][0])) + (asf(st[2][0]) + asf(st[3][0])), (asf(st[0][1]) + asf(st[1][1])) + (asf(st[2][1]) + asf(st[3][1])), h, rs, sd, xa);
          USE(xa); USE(rs); USE(sd);
          g1[1] = lds128(lds + T_AUG1 + 512 + tok * 16); g1[0] = lds128(lds + T_AUG1 + tok * 16);
        }
        if constexpr (s == 10) { a0 = mfma(g1[0], xa, a0); a1 = mfma(g1[1], xa, a1); USE(a0); USE(a1); }
        SB();
      });
      ST(12);
      asm volatile("s_barrier" ::: "memory");
      SB();
      const float c0 = (-2.f * 1.4426950408889634f * 0.7978845608028654f) * rs, c1 = c0 * 0.044715f * rs * rs;
      f32x16 a2, a3;
      auto rdf2 = [&](auto M_) {
        constexpr int m = M_.value;
        if constexpr (m < 32) f2[m] = lds128(pb + m * 1024);
        else if constexpr (m < 64) f2[m] = lds128(lds + vh + (m & 1) * 256 + ((m - 32) >> 1) * 1024);
      };
      auto mstep = [&](auto M_) {
        constexpr int m = M_.value;
        if constexpr (m < 64) {
          SB();
          if constexpr (m < 32) { a2 = mfma(f2[m], x[m], a2); USE(a2); } else { a3 = mfma(f2[m], x[m - 32], a3); USE(a3); }
          if constexpr ((m & 1) == 0) { rdf2(IC(m + 5)); rdf2(IC(m + 4)); }
          SB();
        }
      };
      a2 = mfma(g1[2], xa, zero16()); USE(a2);
      asm volatile("s_nop 7");
      SB();
      v4u ho;
      auto group = [&](const f32x16& a, auto T_, auto G_, auto M_, auto N_) {
        constexpr int t = T_.value, g = G_.value, M = M_.value, N = N_.value;
        const float v0 = a[4 * g], v1 = a[4 * g + 1], v2 = a[4 * g + 2], v3 = a[4 * g + 3];
        float u0, u1, u2, u3;
        mstep(IC(M));
        asm volatile("v_mul_f32 %0, %4, %4\n\tv_mul_f32 %1, %5, %5\n\tv_mul_f32 %2, %6, %6\n\tv_mul_f32 %3, %7, %7\n\tv_fma_f32 %0, %0, %8, %9"
                     : "=&v"(u0), "=&v"(u1), "=&v"(u2), "=&v"(u3) : "v"(v0), "v"(v1), "v"(v2), "v"(v3), "v"(c1), "v"(c0));
        mstep(IC(M + 1));
        asm volatile("v_fma_f32 %1, %1, %6, %7\n\tv_fma_f32 %2, %2, %6, %7\n\tv_fma_f32 %3, %3, %6, %7\n\tv_mul_f32 %0, %0, %4\n\tv_mul_f32 %1, %1, %5"
                     : "+v"(u0), "+v"(u1), "+v"(u2), "+v"(u3) : "v"(v0), "v"(v1), "v"(c1), "v"(c0));
        mstep(IC(M + 2));
        asm volatile("v_mul_f32 %2, %2, %4\n\tv_mul_f32 %3, %3, %5\n\tv_exp_f32 %0, %0\n\tv_exp_f32 %1, %1" : "+v"(u0), "+v"(u1), "+v"(u2), "+v"(u3) : "v"(v2), "v"(v3));
        mstep(IC(M + 3));
        asm volatile("v_exp_f32 %2, %2\n\tv_exp_f32 %3, %3\n\tv_fma_f32 %0, %0, %4, %4\n\tv_fma_f32 %1, %1, %4, %4\n\tv_fma_f32 %2, %2, %4, %4\n\tv_fma_f32 %3, %3, %4, %4"
                     : "+v"(u0), "+v"(u1), "+v"(u2), "+v"(u3) : "v"(sd));
        mstep(IC(M + 4));
        asm volatile("v_rcp_f32 %0, %0\n\tv_rcp_f32 %1, %1\n\tv_rcp_f32 %2, %2\n\tv_rcp_f32 %3, %3" : "+v"(u0), "+v"(u1), "+v"(u2), "+v"(u3));
        if constexpr (N > 5) mstep(IC(M + 5));
        unsigned o0, o1;
        asm volatile("v_mul_f32 %2, %2, %6\n\tv_mul_f32 %3, %3, %7\n\tv_mul_f32 %4, %4, %8\n\tv_mul_f32 %5, %5, %9\n\tv_cvt_pk_bf16_f32 %0, %2, %3\n\tv_cvt_pk_bf16_f32 %1, %4, %5"
                     : "=&v"(o0), "=&v"(o1), "+v"(u0), "+v"(u1), "+v"(u2), "+v"(u3) : "v"(v0), "v"(v1), "v"(v2), "v"(v3));
        ho[2 * (g & 1)] = o0; ho[2 * (g & 1) + 1] = o1;
        if constexpr (g & 1) st128c(ho, rW, v16 + ((2 * t + (g >> 1)) & 3) * 1024, sh + ((2 * t + (g >> 1)) >> 2) * 4096);
        SB();
      };
      static_for<0, 12>([&](auto G_) {
        constexpr int G = G_.value, m0 = (G * 16) / 3, m1 = ((G + 1) * 16) / 3;
        if constexpr (G < 4) group(a0, IC(0), IC(G), IC(m0), IC(m1 - m0));
        else if constexpr (G < 8) group(a1, IC(1), IC(G - 4), IC(m0), IC(m1 - m0));
        else group(a2, IC(2), IC(G - 8), IC(m0), IC(m1 - m0));
        if constexpr (G == 4) { a3 = mfma(g1[3], xa, zero16()); USE(a3); }
        if constexpr ((G & 1) == 0 && G < 8) dma_kv2_if0<G>(L, rQ, vq, kvs, kvl);
        if constexpr (G & 1) w2_load4_if0<32 + 4 * (G >> 1)>(L, rW, v16, w2s);
        SB();
      });
      asm volatile("s_nop 7");
      group(a3, IC(3), IC(0), IC(64), IC(5)); group(a3, IC(3), IC(1), IC(64), IC(5));
      ST(13);
      WAIT_VM0();
#pragma unroll
      for (int i = 0; i < 7; ++i) dmac(rW, v16, sh + i * 1024, junk);
      BAR();
      if (w == 0) post(rW, lane * 128, OFF_MBX + ((2 * 2 + L) * 256 + pn * 16) * 128 + j * 8, lane < 16, tlo, thi);
      ST(14);
    }
    {
      CTX();
      if (w == 0) poll(rW, lane * 8, OFF_MBX + ((2 * 2 + L) * 256 + cu) * 128, lane < 16, tlo, thi);
      BAR();
      ST(15);
      const unsigned wr0 = LB + w * 8192 + v16, wr1 = LB + w * 8192 + (v16 ^ 128);
      const unsigned rd = LB + fc * 1024 + (((w * 8 + ft) ^ ((fc & 1) << 3)) * 16);
      constexpr int W2 = WINH;
      const rsrc_t rOut = mk_rsrc(P->out, 2048 * 512 * 2);
      v4u xh[56];
      const unsigned sh = OFF_H + L * 7 * MB + (pn * 4 + th * 2) * 114688 + w * 28672;
#define LDH(i) LDF(rW, v16, sh + ((i) / 28) * 114688, (i) % 28)
#pragma unroll
      for (int i = 0; i < W2; ++i) xh[i] = LDH(i);
      const f32x4 blo = ldsf4(lds + T_B2 + fc * 32), bhi = ldsf4(lds + T_B2 + fc * 32 + 16);
      SB();
      f32x16 a0, a1, a2, a3;
      static_for<0, 28>([&](auto S_) {
        constexpr int s = S_.value;
        xh[s + W2] = LDH(s + W2);
        w2_mfma<2 * s, s == 0>(a0, xh[s]); w2_mfma<2 * s + 1, s == 0>(a1, xh[s]);
        SB();
      });
      static_for<0, 28>([&](auto S_) {
        constexpr int s = S_.value;
        if constexpr (28 + s + W2 < 56) xh[28 + s + W2] = LDH(28 + s + W2);
        w2_mfma<2 * s, s == 0>(a2, xh[28 + s]); w2_mfma<2 * s + 1, s == 0>(a3, xh[28 + s]);
        if constexpr (s >= 8 && s < 16) red_write(lds, wr0, wr1, ((s - 8) >> 2) * 4096, s < 12 ? a0 : a1, (s - 8) & 3);
        SB();
      });
      asm volatile("s_nop 11" : "+v"(a2), "+v"(a3));
#pragma unroll
      for (int g = 0; g < 4; ++g) red_write(lds, wr0, wr1, 32768, a2, g);
#pragma unroll
      for (int g = 0; g < 4; ++g) red_write(lds, wr0, wr1, 32768 + 4096, a3, g);
      BAR();
      ST(16);
      FINALIZE(true, OFF_X2, OFF_ST + 2 * 131072)
      BAR();
      if (w == 0) post(rW, lane * 128, OFF_MBX + ((3 * 2 + 0) * 256 + pn * 16) * 128 + j * 8, lane < 16, tlo, thi);
      const rsrc_t rQ = mk_rsrc(P->wqkv, 512 * 1536 * 2);
#pragma unroll
      for (int i = 0; i < 8; ++i) { const int s = w * 8 + i; dma(rQ, vq, s * 16 * 3072 + j * 64, LB + s * 1024); }
      ST(18);
      asm volatile(".Lfused_end:");
    }
    {
      CTX();
      if (w == 0) poll(rW, lane * 8, OFF_MBX + ((3 * 2 + 0) * 256 + cu) * 128, lane < 16, tlo, thi);
      BAR();
      L = 1; ST(1);
      aK = zero16(); aV = zero16();
      const unsigned sx = OFF_X2 + gtb * 32768;
      v2u st[4];
      static_for<0, 4>([&](auto S_) { constexpr int s = S_.value; x[s] = ALDF(rW, v16, sx, s); });
      static_for<0, 4>([&](auto I_) { constexpr int i = I_.value; st[i] = ald64<i * 256>(rW, (unsigned)(h * 1024 + tok * 8), OFF_ST + 2 * 131072 + gtb * 2048); });
      static_for<4, WIN>([&](auto S_) { constexpr int s = S_.value; x[s] = ALDF(rW, v16, sx, s); });
      LAS unsigned char* pa = lds + LA + trb;
      v4u wk[32], wv[32];
#pragma unroll
      for (int s = 0; s < KD; ++s) { wv[s] = trread(pa + 32768 + s * 1024); wk[s] = trread(pa + s * 1024); }
      SB();
      static_for<0, 32>([&](auto S_) {
        constexpr int s = S_.value;
        WAITV((s + WIN <= 32 ? WIN - 1 : 31 - s) + (s < 4 ? 4 : 0), x[s]);
        aK = mfma(wk[s], x[s], aK);
        USE(aK);
        if constexpr (s + WIN < 32) x[s + WIN] = ALDF(rW, v16, sx, s + WIN);
        aV = mfma(x[s], wv[s], aV);
        USE(aV);
        if constexpr (s + KD < 32) { wv[s + KD] = trread(pa + 32768 + (s + KD) * 1024); wk[s + KD] = trread(pa + (s + KD) * 1024); }
        if constexpr (s == 6) {
          asm volatile("" : "+v"(st[0]), "+v"(st[1]), "+v"(st[2]), "+v"(st[3]));
          float sd;
          ln_stats((asf(st[0][0]) + asf(st[1][0])) + (asf(st[2][0]) + asf(st[3][0])), (asf(st[0][1]) + asf(st[1][1])) + (asf(st[2][1]) + asf(st[3][1])), h, rstd, sd, xa);
          USE(xa); USE(rstd);
        }
        SB();
      });
    }
  }
}
extern "C" __global__ void __launch_bounds__(256, 1) fused(Params P) {
  asm volatile("s_branch fused_body" ::: "v255", "a255", "s101", "vcc");
}
}

extern "C" void kernel_launch(void* const* d_in, const int* in_sizes, int n_in, void* d_out, int out_size, void* d_ws, size_t ws_size, hipStream_t stream) {
  Params p;
  p.x = (const unsigned char*)d_in[0]; p.wqkv = (const unsigned char*)d_in[1]; p.wo = (const unsigned char*)d_in[2];
  p.w1 = (const unsigned char*)d_in[4]; p.b1 = (const unsigned char*)d_in[5]; p.w2 = (const unsigned char*)d_in[6]; p.b2 = (const unsigned char*)d_in[7];
  p.out = (unsigned char*)d_out; p.ws = (unsigned char*)d_ws;
  (void)hipFuncSetAttribute((const void*)fused, hipFuncAttributeMaxDynamicSharedMemorySize, LDS_BYTES);
  fused<<<256, 256, LDS_BYTES, stream>>>(p);
}
```

```cpp
#include <hip/hip_runtime.h>
#include <stdint.h>
#include <type_traits>

#define WINX 10
#define WINH 12
#define TOUCH 1
#define TLBWARM 0
#define QFLAG 11
#define PRET 1
#define WINO 8
#define ROLE2 1
#define WARM 1
#define OUTAUX " sc1"
#define MAP48 1
namespace {
#define DI __device__ __forceinline__
#define GAS __attribute__((address_space(1)))
#define LAS __attribute__((address_space(3)))
#define CAS __attribute__((address_space(4)))
typedef unsigned v4u __attribute__((ext_vector_type(4)));
typedef unsigned v2u __attribute__((ext_vector_type(2)));
typedef float f32x16 __attribute__((ext_vector_type(16)));
typedef float f32x4 __attribute__((ext_vector_type(4)));
typedef float f32x2 __attribute__((ext_vector_type(2)));
typedef __bf16 bf16x8 __attribute__((ext_vector_type(8)));
typedef __bf16 bf16x4 __attribute__((ext_vector_type(4)));
typedef __bf16 bf16x2 __attribute__((ext_vector_type(2)));
typedef unsigned long long u64;
typedef GAS u64 gu64;
typedef __amdgpu_buffer_rsrc_t rsrc_t;
#define RLX_AGENT __ATOMIC_RELAXED, __HIP_MEMORY_SCOPE_AGENT

constexpr unsigned MB = 1u << 20;
constexpr unsigned OFF_EPOCH = 0, OFF_HELLO = 4096, OFF_MBX = 8192;
constexpr unsigned OFF_WQT = 46 * MB;
constexpr unsigned OFF_FKV = 48 * MB;
constexpr unsigned OFF_FH = OFF_FKV + 262144;
constexpr unsigned OFF_DBG = 512 * 1024;
constexpr unsigned OFF_ST = 640 * 1024;
constexpr unsigned OFF_WOT = 1 * MB;
constexpr unsigned OFF_W1T = OFF_WOT + 512 * 1024;
constexpr unsigned OFF_W2T = OFF_W1T + 1792 * 1024;
constexpr unsigned OFF_X2 = OFF_W2T + 1792 * 1024;
constexpr unsigned OFF_X1 = OFF_X2 + 2 * MB;
constexpr unsigned OFF_O = OFF_X1 + 4 * MB;
constexpr unsigned OFF_KV = OFF_O + 4 * MB;
constexpr unsigned OFF_H = OFF_KV + 8 * MB;
constexpr unsigned WS_BYTES = 50 * MB;
constexpr unsigned LA = 0, LB = 65536, LC = 131072, LT = 147456, LDS_BYTES = 163840;
constexpr unsigned T_AUGQ = LT;
constexpr unsigned T_AUG1 = LT + 2048;
constexpr unsigned T_STAT = LT + 4096;
constexpr unsigned T_B2 = LT + 4608;
constexpr unsigned T_PART = LT + 5120;
constexpr unsigned T_JUNK = LT + 8192;
constexpr unsigned ZOFF = 0x20000;
constexpr unsigned TS = 4128;
constexpr unsigned SPIN_CAP = 1u << 18;

struct Params {
  const unsigned char* x; const unsigned char* wqkv; const unsigned char* wo; const unsigned char* w1;
  const unsigned char* b1; const unsigned char* w2; const unsigned char* b2; unsigned char* out; unsigned char* ws;
};

DI float asf(unsigned u) { return __builtin_bit_cast(float, u); }
DI unsigned asu(float f) { return __builtin_bit_cast(unsigned, f); }
DI bf16x2 asb2(unsigned u) { return __builtin_bit_cast(bf16x2, u); }
DI rsrc_t mk_rsrc(const void* p, unsigned bytes) { return __builtin_amdgcn_make_buffer_rsrc((void*)p, 0, bytes, 0x00020000); }
DI v4u ld128(rsrc_t r, unsigned voff, unsigned soff) { return __builtin_amdgcn_raw_buffer_load_b128(r, voff, soff, 0); }
DI v4u ld128c(rsrc_t r, unsigned voff, unsigned soff) { return __builtin_amdgcn_raw_buffer_load_b128(r, voff, soff, 16); }
DI v2u ld64c(rsrc_t r, unsigned voff, unsigned soff) { return __builtin_amdgcn_raw_buffer_load_b64(r, voff, soff, 16); }
DI void st128c(v4u v, rsrc_t r, unsigned voff, unsigned soff) { __builtin_amdgcn_raw_buffer_store_b128(v, r, voff, soff, 16); asm volatile("s_nop 1" ::"v"(v)); }
DI void st64c(v2u v, rsrc_t r, unsigned voff, unsigned soff) { __builtin_amdgcn_raw_buffer_store_b64(v, r, voff, soff, 16); }
#define LDF(r, v16, sbase, i) ld128c(r, (v16) + ((i) & 3) * 1024u, (sbase) + ((i) >> 2) * 4096u)

DI void dma(rsrc_t r, unsigned voff, unsigned soff, unsigned lds) {
  asm volatile("s_mov_b32 m0, %3\n\ts_nop 0\n\tbuffer_load_dwordx4 %0, %1, %2 offen lds" ::"v"(voff), "s"(r), "s"(soff), "s"(lds));
}
DI void dmac(rsrc_t r, unsigned voff, unsigned soff, unsigned lds) {
  asm volatile("s_mov_b32 m0, %3\n\ts_nop 0\n\tbuffer_load_dwordx4 %0, %1, %2 offen sc1 lds" ::"v"(voff), "s"(r), "s"(soff), "s"(lds));
}
DI void dmac4(rsrc_t r, unsigned voff, unsigned soff, unsigned lds) {
  asm volatile("s_mov_b32 m0, %3\n\ts_nop 0\n\tbuffer_load_dwordx4 %0, %1, %2 offen sc1 lds\n\tbuffer_load_dwordx4 %0, %1, %2 offen offset:1024 sc1 lds\n\t"
               "buffer_load_dwordx4 %0, %1, %2 offen offset:2048 sc1 lds\n\tbuffer_load_dwordx4 %0, %1, %2 offen offset:3072 sc1 lds" ::"v"(voff), "s"(r), "s"(soff), "s"(lds));
}
DI void dma_if0(int skip, rsrc_t r, unsigned voff, unsigned soff, unsigned lds) {
  asm volatile("s_cmp_lg_u32 %4, 0\n\ts_cbranch_scc1 1f\n\ts_mov_b32 m0, %3\n\ts_nop 0\n\tbuffer_load_dwordx4 %0, %1, %2 offen lds\n1:" ::"v"(voff), "s"(r), "s"(soff), "s"(lds), "s"(skip) : "scc");
}
template <int OFF> DI v4u ald128(rsrc_t r, unsigned voff, unsigned soff) {
  v4u v; asm volatile("buffer_load_dwordx4 %0, %1, %2, %3 offen offset:%c4 sc1" : "=v"(v) : "v"(voff), "s"(r), "s"(soff), "i"(OFF)); return v;
}
template <int OFF> DI v2u ald64(rsrc_t r, unsigned voff, unsigned soff) {
  v2u v; asm volatile("buffer_load_dwordx2 %0, %1, %2, %3 offen offset:%c4 sc1" : "=v"(v) : "v"(voff), "s"(r), "s"(soff), "i"(OFF)); return v;
}
#define ALDF(r, v16, sbase, i) ald128<((i) & 3) * 1024>(r, v16, (sbase) + ((i) >> 2) * 4096u)
#define WAITV(n, x) asm volatile("s_waitcnt vmcnt(%1)" : "+v"(x) : "n"(n))
DI void touch(rsrc_t r, unsigned voff, unsigned soff, unsigned lds) {
  asm volatile("s_mov_b32 m0, %3\n\ts_nop 0\n\tbuffer_load_dword %0, %1, %2 offen lds" ::"v"(voff), "s"(r), "s"(soff), "s"(lds));
}
template <int S> DI void dma_kv2_if0(int skip, rsrc_t r, unsigned voff, unsigned soff, unsigned lds) {
  unsigned t;
  asm volatile("s_cmp_lg_u32 %5, 0\n\ts_cbranch_scc1 1f\n\t"
               "s_add_u32 m0, %4, %c6\n\ts_add_u32 %0, %3, %c7\n\tbuffer_load_dwordx4 %1, %2, %0 offen lds\n\t"
               "s_add_u32 m0, m0, 31744\n\ts_nop 0\n\tbuffer_load_dwordx4 %1, %2, %0 offen offset:1024 lds\n\t"
               "s_add_u32 m0, m0, 1024\n\ts_add_u32 %0, %0, 49152\n\tbuffer_load_dwordx4 %1, %2, %0 offen offset:1024 lds\n\t"
               "s_sub_u32 m0, m0, 31744\n\ts_nop 0\n\tbuffer_load_dwordx4 %1, %2, %0 offen lds\n1:"
               : "=&s"(t) : "v"(voff), "s"(r), "s"(soff), "s"(lds), "s"(skip), "i"(S * 1024), "i"(S * 49152) : "scc");
}
DI void dmac4j(rsrc_t r, unsigned voff, unsigned soff, unsigned lds) {
  asm volatile("s_mov_b32 m0, %3\n\ts_nop 0\n\tbuffer_load_dwordx4 %0, %1, %2 offen sc1 lds\n\ts_sub_u32 m0, m0, 1024\n\ts_nop 0\n\tbuffer_load_dwordx4 %0, %1, %2 offen offset:1024 sc1 lds\n\t"
               "s_sub_u32 m0, m0, 1024\n\ts_nop 0\n\tbuffer_load_dwordx4 %0, %1, %2 offen offset:2048 sc1 lds\n\ts_sub_u32 m0, m0, 1024\n\ts_nop 0\n\tbuffer_load_dwordx4 %0, %1, %2 offen offset:3072 sc1 lds"
               ::"v"(voff), "s"(r), "s"(soff), "s"(lds) : "scc");
}
template <int OFF> DI void dmac2_if(int go, rsrc_t r, unsigned voff, unsigned soff, unsigned lds) {
  unsigned t;
  asm volatile("s_cmp_eq_u32 %5, 0\n\ts_cbranch_scc1 1f\n\ts_add_u32 m0, %4, %c6\n\ts_add_u32 %0, %3, %c6\n\tbuffer_load_dwordx4 %1, %2, %0 offen sc1 lds\n\t"
               "buffer_load_dwordx4 %1, %2, %0 offen offset:1024 sc1 lds\n1:" : "=&s"(t) : "v"(voff), "s"(r), "s"(soff), "s"(lds), "s"(go), "i"(OFF) : "scc");
}
template <int SO, int LO> DI void dma_o(rsrc_t r, unsigned voff, unsigned soff, unsigned lds) {
  unsigned t;
  asm volatile("s_add_u32 m0, %4, %c6\n\ts_add_u32 %0, %3, %c5\n\tbuffer_load_dwordx4 %1, %2, %0 offen lds" : "=&s"(t) : "v"(voff), "s"(r), "s"(soff), "s"(lds), "i"(SO), "i"(LO) : "scc");
}
template <int OFF> DI void dmac2(rsrc_t r, unsigned voff, unsigned soff, unsigned lds) {
  unsigned t;
  asm volatile("s_add_u32 m0, %4, %c5\n\ts_add_u32 %0, %3, %c5\n\tbuffer_load_dwordx4 %1, %2, %0 offen sc1 lds\n\t"
               "buffer_load_dwordx4 %1, %2, %0 offen offset:1024 sc1 lds" : "=&s"(t) : "v"(voff), "s"(r), "s"(soff), "s"(lds), "i"(OFF) : "scc");
}
template <int SO, int LO> DI void dmac_o(rsrc_t r, unsigned voff, unsigned soff, unsigned lds) {
  asm volatile("s_add_u32 m0, %3, %c5\n\ts_nop 0\n\tbuffer_load_dwordx4 %0, %1, %2 offen offset:%c4 sc1 lds" ::"v"(voff), "s"(r), "s"(soff), "s"(lds), "i"(SO), "i"(LO - SO) : "scc");
}
template <int OFF> DI void dmac4_if0(int skip, rsrc_t r, unsigned voff, unsigned soff, unsigned lds) {
  unsigned t;
  asm volatile("s_cmp_lg_u32 %5, 0\n\ts_cbranch_scc1 1f\n\ts_add_u32 m0, %4, %c6\n\ts_add_u32 %0, %3, %c6\n\tbuffer_load_dwordx4 %1, %2, %0 offen sc1 lds\n\t"
               "buffer_load_dwordx4 %1, %2, %0 offen offset:1024 sc1 lds\n\tbuffer_load_dwordx4 %1, %2, %0 offen offset:2048 sc1 lds\n\t"
               "buffer_load_dwordx4 %1, %2, %0 offen offset:3072 sc1 lds\n1:" : "=&s"(t) : "v"(voff), "s"(r), "s"(soff), "s"(lds), "s"(skip), "i"(OFF) : "scc");
}
#define WAIT_VM0() asm volatile("s_waitcnt vmcnt(0)" ::: "memory")
#define BAR() asm volatile("s_waitcnt lgkmcnt(0)\n\ts_barrier" ::: "memory")
#define SB() __builtin_amdgcn_sched_barrier(0)

DI f32x16 mfma(v4u a, v4u b, f32x16 c) {
  return __builtin_amdgcn_mfma_f32_32x32x16_bf16(__builtin_bit_cast(bf16x8, a), __builtin_bit_cast(bf16x8, b), c, 0, 0, 0);
}
DI v4u trread(LAS unsigned char* p) {
  bf16x4 a = __builtin_amdgcn_ds_read_tr16_b64_v4bf16((LAS bf16x4*)p);
  bf16x4 b = __builtin_amdgcn_ds_read_tr16_b64_v4bf16((LAS bf16x4*)(p + 256));
  return __builtin_bit_cast(v4u, __builtin_shufflevector(a, b, 0, 1, 2, 3, 4, 5, 6, 7));
}
DI v4u lds128(LAS unsigned char* p) { return *(LAS v4u*)p; }
DI f32x4 ldsf4(LAS unsigned char* p) { return *(LAS f32x4*)p; }
DI unsigned pk(float a, float b) { f32x2 v = {a, b}; return __builtin_bit_cast(unsigned, __builtin_convertvector(v, bf16x2)); }
DI float bflo(unsigned u) { return __builtin_bit_cast(float, u << 16); }
DI float bfhi(unsigned u) { return __builtin_bit_cast(float, u & 0xffff0000u); }
DI float xhalf_add(float v) { auto r = __builtin_amdgcn_permlane32_swap(__builtin_bit_cast(unsigned, v), __builtin_bit_cast(unsigned, v), false, false);
  return asf(r[0]) + asf(r[1]); }
DI float xhalf_max(float v) { auto r = __builtin_amdgcn_permlane32_swap(__builtin_bit_cast(unsigned, v), __builtin_bit_cast(unsigned, v), false, false);
  return fmaxf(asf(r[0]), asf(r[1])); }
DI float x16_add(float v) { auto r = __builtin_amdgcn_permlane16_swap(__builtin_bit_cast(unsigned, v), __builtin_bit_cast(unsigned, v), false, false);
  return asf(r[0]) + asf(r[1]); }
DI float x8_add(float v) { return v + __builtin_bit_cast(float, __builtin_amdgcn_update_dpp(0, __builtin_bit_cast(int, v), 0x128, 0xf, 0xf, false)); }
DI f32x16 zero16() { f32x16 z; for (int i = 0; i < 16; ++i) z[i] = 0.f; return z; }

DI void poll_rest(v2u f, rsrc_t r, unsigned voff, unsigned soff, unsigned tlo, unsigned thi) {
  unsigned n = SPIN_CAP;
  asm volatile("1:\n\tv_cmp_ne_u64 vcc, %5, %0\n\ts_cbranch_vccz 2f\n\ts_sub_u32 %1, %1, 1\n\ts_cbranch_scc1 2f\n\t"
               "buffer_load_dwordx2 %0, %2, %3, %4 offen sc1\n\ts_waitcnt vmcnt(0)\n\ts_branch 1b\n2:"
               : "+v"(f), "+s"(n) : "v"(voff), "s"(r), "s"(soff), "s"((u64)thi << 32 | tlo) : "vcc", "scc", "memory");
}
DI void poll(rsrc_t r, unsigned voff, unsigned soff, unsigned tlo, unsigned thi) { poll_rest(v2u{~tlo, thi}, r, voff, soff, tlo, thi); }
DI void post(rsrc_t r, unsigned voff, unsigned soff, bool active, unsigned tlo, unsigned thi) { st64c(v2u{tlo, thi}, r, active ? voff : 0x80000000u, soff); }

#define ST(i)

template <int I, int N, class F> DI void static_for(F&& f) { if constexpr (I < N) { f(std::integral_constant<int, I>{}); static_for<I + 1, N>(f); } }
#define IC(n) std::integral_constant<int, n>{}
#define USE(x) asm volatile("" ::"v"(x))
DI float mulf(float a, float b) { float r; asm("v_mul_f32 %0, %1, %2" : "=v"(r) : "v"(a), "v"(b)); return r; }
DI float fmaf_(float a, float b, float c) { float r; asm("v_fma_f32 %0, %1, %2, %3" : "=v"(r) : "v"(a), "v"(b), "v"(c)); return r; }
DI float ex2(float x) { return __builtin_amdgcn_exp2f(x); }
DI float max3(float a, float b, float c) { return fmaxf(fmaxf(a, b), c); }
DI unsigned dup16(unsigned lo16) { return lo16 | (lo16 << 16); }

DI void ln_stats(float S, float Q, int h, float& rstd, float& sd, v4u& xa) {
  S = xhalf_add(S); Q = xhalf_add(Q);
  const float mean = S * (1.f / 512.f), var = fmaxf(__builtin_fmaf(-mean, mean, Q * (1.f / 512.f)), 0.f) + 1e-5f;
  rstd = __builtin_amdgcn_rsqf(var); sd = var * rstd;
  const unsigned hi = pk(-mean, sd);
  const unsigned lo = pk(-mean - bflo(hi), sd - bfhi(hi));
  const unsigned m = (hi & 0xffffu) | (lo << 16), s = (hi >> 16) | (lo & 0xffff0000u);
  xa = h ? v4u{0, 0, 0, 0} : v4u{m, m, s, 0};
}
DI v4u aug_entry(float cs, unsigned b16) {
  const unsigned hi = pk(cs, 0.f) & 0xffffu, lo = pk(cs - bflo(hi), 0.f) & 0xffffu;
  return v4u{dup16(hi), dup16(lo), dup16(b16), 0};
}

DI f32x16 attn_exact(LAS unsigned char* kp, LAS unsigned char* vp, v4u q, int dq) {
  const f32x16 z = zero16();
  float m = -3.0e38f;
#pragma unroll 1
  for (int d = 0; d < 9; ++d) {
    const f32x16 s = mfma(lds128(kp + d * TS), q, z);
    const int hi = d == 8 ? dq : 99, lo = d == 0 ? dq : -99;
#pragma unroll
    for (int r = 0; r < 16; ++r) { const int i = (r & 3) + 8 * (r >> 2); m = (i <= hi && i > lo) ? fmaxf(m, s[r]) : m; }
  }
  m = xhalf_max(m);
  f32x16 o = z;
#pragma unroll 1
  for (int d = 0; d < 9; ++d) {
    const f32x16 s = mfma(lds128(kp + d * TS), q, z);
    const int hi = d == 8 ? dq : 99, lo = d == 0 ? dq : -99;
    float e[16];
#pragma unroll
    for (int r = 0; r < 16; ++r) { const int i = (r & 3) + 8 * (r >> 2); e[r] = (i <= hi && i > lo) ? ex2(s[r] - m) : 0.f; }
    o = mfma(lds128(vp + d * TS), v4u{pk(e[0], e[1]), pk(e[2], e[3]), pk(e[4], e[5]), pk(e[6], e[7])}, o);
    o = mfma(lds128(vp + d * TS + 1040), v4u{pk(e[8], e[9]), pk(e[10], e[11]), pk(e[12], e[13]), pk(e[14], e[15])}, o);
  }
  return o;
}
template <int HD, class Hook>
DI v4u attn_unit(LAS unsigned char* kp, LAS unsigned char* vp, v4u q, const unsigned (&pm)[8], int dq, Hook&& hook) {
  const f32x16 z = zero16();
  f32x16 o, s;
  v4u ph, va, vb, kn;
  {
    const v4u kd = lds128(kp + 8 * TS), kf = lds128(kp), k1 = lds128(kp + TS);
    const v4u vd0 = lds128(vp + 8 * TS), vf0 = lds128(vp), vd1 = lds128(vp + 8 * TS + 1040);
    vb = lds128(vp + 1040); va = lds128(vp + TS); kn = lds128(kp + 2 * TS);
    f32x16 sd = mfma(kd, q, z), sf = mfma(kf, q, z);
    s = mfma(k1, q, z);
    USE(sd); USE(sf); USE(s);
    SB();
    hook(IC(0));
    unsigned p[8];
#pragma unroll
    for (int r = 0; r < 8; r += 2) { const int i0 = (r & 3) + 8 * (r >> 2); p[r >> 1] = pk(ex2((i0 <= dq) ? sd[r] : sf[r]), ex2((i0 + 1 <= dq) ? sd[r + 1] : sf[r + 1])); }
    const v4u pd0 = {p[0] & pm[0], p[1] & pm[1], p[2] & pm[2], p[3] & pm[3]}, pf0 = {p[0] & ~pm[0], p[1] & ~pm[1], p[2] & ~pm[2], p[3] & ~pm[3]};
    USE(pd0); USE(pf0);
    SB();
    o = mfma(vd0, pd0, z);
    USE(o);
    SB();
#pragma unroll
    for (int r = 8; r < 12; r += 2) { const int i0 = (r & 3) + 8 * (r >> 2); p[r >> 1] = pk(ex2((i0 <= dq) ? sd[r] : sf[r]), ex2((i0 + 1 <= dq) ? sd[r + 1] : sf[r + 1])); }
    USE(p[4]); USE(p[5]);
    SB();
    o = mfma(vf0, pf0, o);
    USE(o);
    SB();
#pragma unroll
    for (int r = 12; r < 16; r += 2) { const int i0 = (r & 3) + 8 * (r >> 2); p[r >> 1] = pk(ex2((i0 <= dq) ? sd[r] : sf[r]), ex2((i0 + 1 <= dq) ? sd[r + 1] : sf[r + 1])); }
    const v4u pd1 = {p[4] & pm[4], p[5] & pm[5], p[6] & pm[6], p[7] & pm[7]};
    ph = v4u{p[4] & ~pm[4], p[5] & ~pm[5], p[6] & ~pm[6], p[7] & ~pm[7]};
    USE(pd1); USE(ph);
    SB();
    o = mfma(vd1, pd1, o);
    USE(o);
    SB();
  }
  static_for<1, 8>([&](auto D_) {
    constexpr int d = D_.value;
    float e[16];
    f32x16 sn = s;
#pragma unroll
    for (int i = 0; i < 4; ++i) { e[i] = ex2(s[i]); USE(e[i]); }
    SB();
    o = mfma(vb, ph, o);
    USE(o);
    SB();
#pragma unroll
    for (int i = 4; i < 8; ++i) e[i] = ex2(s[i]);
    const v4u pl = {pk(e[0], e[1]), pk(e[2], e[3]), pk(e[4], e[5]), pk(e[6], e[7])};
    USE(pl);
    SB();
    if constexpr (d + 1 < 8) { sn = mfma(kn, q, z); USE(sn); }
    hook(IC(d));
    SB();
#pragma unroll
    for (int i = 8; i < 12; ++i) { e[i] = ex2(s[i]); USE(e[i]); }
    SB();
    o = mfma(va, pl, o);
    USE(o);
    if constexpr (d + 1 < 8) va = lds128(vp + (d + 1) * TS);
    if constexpr (d + 2 < 8) kn = lds128(kp + (d + 2) * TS);
    vb = lds128(vp + d * TS + 1040);
    SB();
#pragma unroll
    for (int i = 12; i < 16; ++i) e[i] = ex2(s[i]);
    ph = v4u{pk(e[8], e[9]), pk(e[10], e[11]), pk(e[12], e[13]), pk(e[14], e[15])};
    USE(ph);
    s = sn;
    SB();
  });
  o = mfma(vb, ph, o);
  {
    const float l = o[8 - 8 * HD];
    const float a = max3(max3(fabsf(o[8 * HD]), fabsf(o[8 * HD + 1]), fabsf(o[8 * HD + 2])), max3(fabsf(o[8 * HD + 3]), fabsf(o[8 * HD + 4]), fabsf(o[8 * HD + 5])),
                         fabsf(o[8 * HD + 6]) + fabsf(o[8 * HD + 7]));
    const float t = ((o[8 * HD] + o[8 * HD + 1]) + (o[8 * HD + 2] + o[8 * HD + 3])) + (o[8 * HD + 4] + o[8 * HD + 5]);
    const bool safe = l > 1e-24f && l < 1e30f && a < 1e36f && t == t;
    if (__builtin_expect(!__all(safe), 0)) o = attn_exact(kp, vp, q, dq);
  }
  const float inv = __builtin_amdgcn_rcpf(o[8 - 8 * HD]);
  v4u r;
#pragma unroll
  for (int i = 0; i < 4; ++i) r[i] = pk(o[8 * HD + 2 * i] * inv, o[8 * HD + 2 * i + 1] * inv);
  return r;
}

#define CTX() \
  int b = b0, w = w0, lane = lane0; unsigned tlo = tag_lo, thi = tag_hi; const CAS Params* P = P0; \
  asm volatile("" : "+s"(b), "+s"(w), "+s"(tlo), "+s"(thi), "+s"(P)); asm volatile("" : "+v"(lane)); \
  const int tid = w * 64 + lane, pn = MAP48 ? 4 * ((b & 7) >> 1) + ((b >> 3) & 3) : 2 * (b & 7) + (b >> 7), j = MAP48 == 2 ? ((b >> 6) << 2) | ((b & 1) << 1) | ((b >> 5) & 1) : MAP48 ? 8 * (b & 1) + (b >> 5) : (b >> 3) & 15, cu = pn * 16 + j, h = lane >> 5, tok = lane & 31; \
  const unsigned v16 = lane * 16; const int pn2 = ROLE2 ? 2 * (b & 7) + (b >> 7) : pn, j2 = ROLE2 ? (b >> 3) & 15 : j, cu2 = pn2 * 16 + j2, th = j2 & 1, ng = j2 >> 1, ft = lane & 7, fc = lane >> 3, gtb = pn * 4 + w; \
  const unsigned trb = (8 * h + ((lane & 15) >> 2)) * 64 + ((lane >> 4) & 1) * 32 + (lane & 3) * 8; \
  const unsigned vq = (lane >> 2) * 3072 + (lane & 3) * 16, junk = T_JUNK + w * 1024; \
  (void)tid; (void)cu; (void)cu2; (void)tok; (void)v16; (void)th; (void)ng; (void)ft; (void)fc; (void)gtb; (void)trb; (void)vq; (void)junk;

template <int I> DI void w2_load(rsrc_t r, unsigned voff, unsigned soff) {
  asm volatile("buffer_load_dwordx4 a[%c3:%c4], %0, %1, %2 offen offset:%c5 sc1" ::"v"(voff), "s"(r), "s"(soff), "i"(4 * I), "i"(4 * I + 3), "i"((I & 3) * 1024));
}
template <int R, int OFF> DI void acc_load(rsrc_t r, unsigned voff, unsigned soff) {
  asm volatile("buffer_load_dwordx4 a[%c3:%c3+3], %0, %1, %2 offen offset:%c4 sc1" ::"v"(voff), "s"(r), "s"(soff), "i"(R), "i"(OFF));
}
template <int LO, int HI> DI void w2_load_range(rsrc_t r, unsigned voff, unsigned sbase) {
  if constexpr (LO < HI) { w2_load<LO>(r, voff, sbase + (LO >> 2) * 4096); w2_load_range<LO + 1, HI>(r, voff, sbase); }
}
template <int I> DI void w2_load2_if0(int skip, rsrc_t r, unsigned voff, unsigned sbase) {
  asm volatile("s_cmp_lg_u32 %3, 0\n\ts_cbranch_scc1 1f\n\tbuffer_load_dwordx4 a[%c4:%c5], %0, %1, %2 offen offset:%c8 sc1\n\tbuffer_load_dwordx4 a[%c6:%c7], %0, %1, %2 offen offset:%c9 sc1\n1:"
               ::"v"(voff), "s"(r), "s"(sbase + (I >> 2) * 4096), "s"(skip), "i"(4 * I), "i"(4 * I + 3), "i"(4 * I + 4), "i"(4 * I + 7), "i"((I & 3) * 1024), "i"((I & 3) * 1024 + 1024) : "scc");
}
template <int I> DI void w2_load4_if0(int skip, rsrc_t r, unsigned voff, unsigned sbase) {
  unsigned t;
  asm volatile("s_cmp_lg_u32 %4, 0\n\ts_cbranch_scc1 1f\n\ts_add_u32 %0, %3, %c5\n\tbuffer_load_dwordx4 a[%c6:%c6+3], %1, %2, %0 offen sc1\n\t"
               "buffer_load_dwordx4 a[%c6+4:%c6+7], %1, %2, %0 offen offset:1024 sc1\n\tbuffer_load_dwordx4 a[%c6+8:%c6+11], %1, %2, %0 offen offset:2048 sc1\n\t"
               "buffer_load_dwordx4 a[%c6+12:%c6+15], %1, %2, %0 offen offset:3072 sc1\n1:"
               : "=&s"(t) : "v"(voff), "s"(r), "s"(sbase), "s"(skip), "i"(I * 1024), "i"(4 * I) : "scc");
}
template <int N, int I, bool FIRST> DI void w2_mfma_w(f32x16& acc, v4u& x) {
  if constexpr (FIRST) asm volatile("s_waitcnt vmcnt(%4)\n\tv_mfma_f32_32x32x16_bf16 %0, a[%c2:%c3], %1, 0" : "=&v"(acc) : "v"(x), "i"(4 * I), "i"(4 * I + 3), "n"(N));
  else asm volatile("s_waitcnt vmcnt(%4)\n\tv_mfma_f32_32x32x16_bf16 %0, a[%c2:%c3], %1, %0" : "+v"(acc) : "v"(x), "i"(4 * I), "i"(4 * I + 3), "n"(N));
}
template <int I, bool FIRST> DI void w2_mfma(f32x16& acc, v4u x) {
  if constexpr (FIRST) asm volatile("v_mfma_f32_32x32x16_bf16 %0, a[%c2:%c3], %1, 0" : "=&v"(acc) : "v"(x), "i"(4 * I), "i"(4 * I + 3));
  else asm volatile("v_mfma_f32_32x32x16_bf16 %0, a[%c2:%c3], %1, %0" : "+v"(acc) : "v"(x), "i"(4 * I), "i"(4 * I + 3));
}
DI void red_write(LAS unsigned char* lds, unsigned wr0, unsigned wr1, unsigned off, const f32x16& a, int g) {
  *(LAS f32x4*)(lds + ((g & 1) ? wr1 : wr0) + off + g * 1024) = f32x4{a[4 * g], a[4 * g + 1], a[4 * g + 2], a[4 * g + 3]};
}

extern "C" __global__ void __launch_bounds__(256, 1) fused(Params P) {
  asm volatile("s_getpc_b64 s[8:9]\n.Lfused_pc:\n\ts_mov_b32 s10, .Lfused_end-.Lfused_pc\n\ts_mov_b32 s11, 0x20000\n\ts_and_b32 s9, s9, 0xffff\n\t"
               "s_lshr_b32 s12, s2, 3\n\ts_lshl_b32 s12, s12, 11\n\ts_mov_b32 m0, %0\n\t"
               "v_and_b32 v1, 3, v0\n\tv_lshlrev_b32 v1, 7, v1\n\tv_and_b32 v2, 0xc0, v0\n\tv_lshl_or_b32 v1, v2, 3, v1\n\t"
               "buffer_load_dword v1, s[8:11], s12 offen lds\n\ts_branch fused_body" ::"i"(T_JUNK) : "v255", "a255", "s101", "vcc");
}
extern "C" __global__ void __launch_bounds__(256, 2) fused_body(Params P_) {
  extern __shared__ __attribute__((aligned(16))) unsigned char smem_[];
  LAS unsigned char* lds = (LAS unsigned char*)smem_;
  const int lane0 = threadIdx.x & 63, w0 = __builtin_amdgcn_readfirstlane(threadIdx.x >> 6), b0 = blockIdx.x;
  const CAS Params* P0 = (const CAS Params*)__builtin_amdgcn_kernarg_segment_ptr();
  const rsrc_t rW = mk_rsrc(P0->ws, WS_BYTES);
  int L = 0;
  ST(0);
  unsigned tag_lo = 0, tag_hi = 0;
  v4u res[2];
  const v4u ones = {0x3f803f80u, 0x3f803f80u, 0x3f803f80u, 0x3f803f80u};
  constexpr int WIN = WINX, KD = 3;
  v4u x[32], xa;
  f32x16 aK, aV;
  f32x4 rs4[4];
  float rstd;
  {
    CTX();
    const unsigned char *px = P->x, *pq = P->wqkv, *pw1 = P->w1, *pw2 = P->w2, *pwo = P->wo, *pb2 = P->b2;
    const int xw = (b >> 3) * 4 + w;
    const int gw = b * 4 + w, u1 = gw - 448, isw1 = gw >= 448 && gw < 896;
    const int pks = isw1 ? u1 & 31 : gw < 448 ? gw >> 2 : (gw - 896) >> 2, pcb = isw1 ? u1 >> 5 : gw & 3;
    unsigned pld = isw1 ? 3584 : 1024, ps0 = pks * 16 * pld + pcb * 256, pst1 = isw1 ? 4 * 3584 : 8 * 1024, pst2 = isw1 ? 8 * 3584 : 4 * 1024;
    unsigned pv = (lane >> 4) * pld + (lane & 15) * 16;
    unsigned sx = gtb * 32768, stg = LB + w * 8192, kvs = w * 2 * 49152 + 1024 + j * 64, kvl = LA + w * 2048, qs = w * 8 * 49152 + j * 64;
    unsigned vxs = (lane >> 4) * 1024 + (((lane & 15) ^ (lane >> 4)) * 16), vqq = vq;
    asm volatile("" : "+s"(ps0), "+s"(pst1), "+s"(pst2), "+s"(sx), "+s"(stg), "+s"(kvs), "+s"(kvl), "+s"(qs));
    asm volatile("" : "+v"(pv), "+v"(vxs), "+v"(vqq));
    SB();
    const rsrc_t rX = mk_rsrc(px, 2048 * 512 * 2), rQ = mk_rsrc(pq, 512 * 1536 * 2);
    const unsigned vx4[4] = {vxs, vxs ^ 64, vxs ^ 128, vxs ^ 192};
    const rsrc_t rB2 = mk_rsrc(pb2, 1024);
    const rsrc_t rP = mk_rsrc(isw1 ? pw1 : gw < 448 ? pw2 : pwo, gw < 896 ? 1792 * 1024 : 512 * 1024);
    v2u e; unsigned bt;
    float s0 = 0.f, s1 = 0.f, q0 = 0.f, q1 = 0.f;
    const bf16x2 one2 = asb2(0x3f803f80u);
    f32x16 aC = zero16();
    aK = zero16(); aV = zero16();
    LAS unsigned char* pa = lds + LA + trb;
    LAS unsigned char* po = pa + w * 2048;
    v4u pt[4], ptk, ptq, wk[32], wv[32], ck0, ck1, cv0, cv1, gk, gv;
    const unsigned xr = stg + tok * 256 + ((h ^ (tok & 15)) * 16);
    const unsigned onesk = (lane & 16) ? 0u : 0x3f803f80u, onesv = onesk ^ 0x3f803f80u;
    const v4u selk = {onesk, onesk, onesk, onesk}, selv = {onesv, onesv, onesv, onesv};
#define TOUCH_X() touch(rX, lane < (MAP48 ? 32 : 16) ? lane * 128u : 0x80000000u, MAP48 ? ((b & 7) >> 1) * 524288 + xw * 4096 : (b & 7) * 262144 + xw * 2048, junk)
#define TOUCH_W() touch(rQ, MAP48 ? (lane < 48 ? (lane >> 2) * 1024u + (lane & 3) * (MAP48 == 2 ? 256u : 128u) : 0x80000000u) : (lane >> 4) * 3072u + (lane & 15) * 128u, xw * 4 * 3072 + (MAP48 == 2 ? (b & 1) * 128 : MAP48 ? (b & 1) * 512 : 1024), junk)
#define LOAD_E() e = ald64<0>(rW, 0, OFF_EPOCH); asm volatile("buffer_load_ushort %0, %1, %2, %3 offen" : "=v"(bt) : "v"(lane * 2), "s"(rB2), "s"(ng * 128))
#define STATS(s) _Pragma("unroll") for (int k = 0; k < 4; k += 2) { const bf16x2 u = asb2(x[s][k]), v = asb2(x[s][k + 1]); \
      s0 = __builtin_amdgcn_fdot2_f32_bf16(u, one2, s0, false); s1 = __builtin_amdgcn_fdot2_f32_bf16(v, one2, s1, false); \
      q0 = __builtin_amdgcn_fdot2_f32_bf16(u, u, q0, false); q1 = __builtin_amdgcn_fdot2_f32_bf16(v, v, q1, false); } USE(s0); USE(s1); USE(q0); USE(q1)
#define PT_DMA(i) dma(rP, pv, ps0 + (i & 1) * pst1 + (i >> 1) * pst2, LC + w * 4096 + i * 1024)
#define TOUCH_FLAGS() touch(rW, lane < 8 ? OFF_MBX + (lane * 256 + cu) * 128 : lane < 10 ? OFF_FKV + (((lane & 1) * 256 + cu) * 4 + w) * 128 \
      : (lane >= 16 && lane < 24) ? OFF_FH + ((((lane >> 2) & 1) * 256 + cu) * 4 + w) * 512 + (lane & 3) * 128 \
      : (lane >= 32 && lane < 44) ? OFF_ST + ((lane - 32) >> 2) * 131072 + (pn2 * 4 + th * 2 + ((lane >> 1) & 1)) * 2048 + ng * 256 + (lane & 1) * 128 \
      : lane == 48 ? OFF_HELLO + b * 8 : 0x80000000u, 0, junk)
#define LOAD_PTQ() ptq = ald128<0>(rQ, w < 2 ? vqq : 0x80000000u, (pn * 2 + w) * 49152 + j * 64)
#define LOAD_RES(t) res[t] = ald128<0>(rX, (unsigned)(ft * 1024 + fc * 16), (unsigned)(((pn2 * 4 + th * 2 + t) * 32 + w * 8) * 1024 + ng * 128))
#define PINS() asm volatile("" : "+v"(e), "+v"(res[0]), "+v"(res[1]), "+v"(bt), "+v"(ptq))
#define TABLE() { LAS float* pp = (LAS float*)(lds + T_PART + lane * 4); \
      *(LAS v4u*)(lds + T_AUGQ + lane * 16) = aug_entry((pp[0] + pp[64]) + (pp[128] + pp[192]), 0);     \
      *(LAS float*)(lds + T_B2 + lane * 4) = bflo(bt); }
#define TRF(p, o2) __builtin_bit_cast(v4u, __builtin_shufflevector(__builtin_amdgcn_ds_read_tr16_b64_v4bf16((LAS bf16x4*)(p)), __builtin_amdgcn_ds_read_tr16_b64_v4bf16((LAS bf16x4*)((p) + (o2))), 0, 1, 2, 3, 4, 5, 6, 7))
    const int blk = (lane >> 4) & 1;
    LAS unsigned char* psc = lds + LC + w * 4096 + (8 * h + ((lane & 15) >> 2)) * 256 + blk * 32 + (lane & 3) * 8;
    auto pt_dst = [&](int t) {
      const int c16 = pcb * 8 + 2 * t + blk, jj = (c16 * 147) >> 10, r7 = c16 - 7 * jj;
      const unsigned o1 = OFF_W1T + (jj * 112 + (r7 < 6 ? (r7 >> 1) * 32 + pks : 96 + (pks >> 1))) * 1024 + (((r7 < 6 ? r7 : pks) & 1) * 16 + (lane & 15) + 32 * h) * 16;
      const int nt = pcb * 4 + t;
      const unsigned o2 = (gw < 448 ? OFF_W2T + ((nt >> 1) * 112 + pks) * 2048 : OFF_WOT + ((nt >> 1) * 32 + pks) * 2048) + (nt & 1) * 1024 + v16;
      return isw1 ? o1 : o2;
    };
#define PT_STORE(i) st128c(i < 4 ? pt[i & 3] : i == 4 ? ptk : ptq, rW, i < 4 ? pt_dst(i) : (i == 5 && w >= 2) ? 0x80000000u : v16, \
      i < 4 ? 0 : i == 4 ? OFF_WQT + (j * 96 + pn * 4 + w) * 1024 : OFF_WQT + (j * 96 + 64 + pn * 2 + w) * 1024)
#define LNSTATS() { float sd; ln_stats(s0 + s1, q0 + q1, h, rstd, sd, xa); *(LAS float*)(lds + T_STAT + w * 128 + tok * 4) = rstd; \
      gv = lds128(lds + T_AUGQ + 512 + tok * 16); gk = lds128(lds + T_AUGQ + tok * 16); }
#define RS4() _Pragma("unroll") for (int g = 0; g < 4; ++g) rs4[g] = ldsf4(lds + T_STAT + w * 128 + (8 * g + 4 * h) * 4)
    dma_o<0, 0>(rX, vx4[0], sx, stg);
    dma_o<4096, 1024>(rX, vx4[1], sx, stg);
    dma_o<8192, 2048>(rX, vx4[2], sx, stg);
    dma_o<12288, 3072>(rX, vx4[3], sx, stg);
    dma_o<16384, 4096>(rX, vx4[0], sx, stg);
    dma_o<20480, 5120>(rX, vx4[1], sx, stg);
    dma_o<24576, 6144>(rX, vx4[2], sx, stg);
    dma_o<28672, 7168>(rX, vx4[3], sx, stg);
    dma_o<0, 0>(rQ, vqq, kvs, kvl);
    dma_o<1024, 32768>(rQ, vqq, kvs, kvl);
    dma_o<49152, 1024>(rQ, vqq, kvs, kvl);
    dma_o<50176, 33792>(rQ, vqq, kvs, kvl);
    TOUCH_X();
    dma_o<393216, 8192>(rQ, vqq, kvs, kvl);
    dma_o<394240, 40960>(rQ, vqq, kvs, kvl);
    dma_o<442368, 9216>(rQ, vqq, kvs, kvl);
    dma_o<443392, 41984>(rQ, vqq, kvs, kvl);
    TOUCH_W();
    LOAD_E();
    SB();
    ST(1);
    asm volatile("s_waitcnt vmcnt(12)" ::: "memory");
    x[0] = lds128(lds + (xr ^ 0) + 0);
    x[1] = lds128(lds + (xr ^ 32) + 0);
    x[2] = lds128(lds + (xr ^ 64) + 0);
    x[3] = lds128(lds + (xr ^ 96) + 0);
    x[4] = lds128(lds + (xr ^ 128) + 0);
    x[5] = lds128(lds + (xr ^ 160) + 0);
    x[6] = lds128(lds + (xr ^ 192) + 0);
    x[7] = lds128(lds + (xr ^ 224) + 0);
    SB();
    dma_o<256, 32768>(rX, vx4[0], sx, stg);
    STATS(0);
    SB();
    dma_o<4352, 33792>(rX, vx4[1], sx, stg);
    STATS(1);
    SB();
    dma_o<8448, 34816>(rX, vx4[2], sx, stg);
    STATS(2);
    SB();
    dma_o<12544, 35840>(rX, vx4[3], sx, stg);
    STATS(3);
    SB();
    dma_o<16640, 36864>(rX, vx4[0], sx, stg);
    STATS(4);
    SB();
    dma_o<20736, 37888>(rX, vx4[1], sx, stg);
    STATS(5);
    SB();
    dma_o<24832, 38912>(rX, vx4[2], sx, stg);
    STATS(6);
    SB();
    dma_o<28928, 39936>(rX, vx4[3], sx, stg);
    STATS(7);
    SB();
    asm volatile("s_waitcnt vmcnt(16)" ::: "memory");
    ck0 = trread(po + 0); ck1 = trread(po + 1024); cv0 = trread(po + 32768); cv1 = trread(po + 33792);
    asm volatile("s_barrier" ::: "memory");
    wv[0] = trread(pa + 32768); wk[0] = trread(pa + 0);
    wv[1] = trread(pa + 33792); wk[1] = trread(pa + 1024);
    wv[2] = trread(pa + 34816); wk[2] = trread(pa + 2048);
    SB();
    aC = mfma(selk, ck0, aC); aC = mfma(selk, ck1, aC); aC = mfma(selv, cv0, aC); aC = mfma(selv, cv1, aC); USE(aC);
    SB();
    aK = mfma(wk[0], x[0], aK); USE(aK);
    dma_o<786432, 16384>(rQ, vqq, kvs, kvl);
    SB();
    aV = mfma(x[0], wv[0], aV); USE(aV);
    wv[3] = trread(pa + 35840); wk[3] = trread(pa + 3072);
    SB();
    aK = mfma(wk[1], x[1], aK); USE(aK);
    dma_o<787456, 49152>(rQ, vqq, kvs, kvl);
    SB();
    aV = mfma(x[1], wv[1], aV); USE(aV);
    wv[4] = trread(pa + 36864); wk[4] = trread(pa + 4096);
    SB();
    aK = mfma(wk[2], x[2], aK); USE(aK);
    dma_o<835584, 17408>(rQ, vqq, kvs, kvl);
    SB();
    aV = mfma(x[2], wv[2], aV); USE(aV);
    wv[5] = trread(pa + 37888); wk[5] = trread(pa + 5120);
    SB();
    aK = mfma(wk[3], x[3], aK); USE(aK);
    dma_o<836608, 50176>(rQ, vqq, kvs, kvl);
    SB();
    aV = mfma(x[3], wv[3], aV); USE(aV);
    wv[6] = trread(pa + 38912); wk[6] = trread(pa + 6144);
    SB();
    aK = mfma(wk[4], x[4], aK); USE(aK);
    PT_DMA(0);
    SB();
    aV = mfma(x[4], wv[4], aV); USE(aV);
    wv[7] = trread(pa + 39936); wk[7] = trread(pa + 7168);
    SB();
    aK = mfma(wk[5], x[5], aK); USE(aK);
    TOUCH_FLAGS();
    SB();
    aV = mfma(x[5], wv[5], aV); USE(aV);
    SB();
    aK = mfma(wk[6], x[6], aK); USE(aK);
    PT_DMA(1);
    SB();
    aV = mfma(x[6], wv[6], aV); USE(aV);
    SB();
    aK = mfma(wk[7], x[7], aK); USE(aK);
    LOAD_RES(0);
    SB();
    aV = mfma(x[7], wv[7], aV); USE(aV);
    SB();
    asm volatile("s_waitcnt vmcnt(8)" ::: "memory");
    x[8] = lds128(lds + (xr ^ 0) + 32768);
    x[9] = lds128(lds + (xr ^ 32) + 32768);
    x[10] = lds128(lds + (xr ^ 64) + 32768);
    x[11] = lds128(lds + (xr ^ 96) + 32768);
    x[12] = lds128(lds + (xr ^ 128) + 32768);
    x[13] = lds128(lds + (xr ^ 160) + 32768);
    x[14] = lds128(lds + (xr ^ 192) + 32768);
    x[15] = lds128(lds + (xr ^ 224) + 32768);
    ck0 = trread(po + 8192); ck1 = trread(po + 9216); cv0 = trread(po + 40960); cv1 = trread(po + 41984);
    asm volatile("s_barrier" ::: "memory");
    SB();
    dma_o<512, 0>(rX, vx4[0], sx, stg);
    STATS(8);
    SB();
    dma_o<4608, 1024>(rX, vx4[1], sx, stg);
    STATS(9);
    SB();
    dma_o<8704, 2048>(rX, vx4[2], sx, stg);
    STATS(10);
    SB();
    dma_o<12800, 3072>(rX, vx4[3], sx, stg);
    STATS(11);
    SB();
    dma_o<16896, 4096>(rX, vx4[0], sx, stg);
    STATS(12);
    SB();
    dma_o<20992, 5120>(rX, vx4[1], sx, stg);
    STATS(13);
    wv[8] = trread(pa + 40960); wk[8] = trread(pa + 8192);
    SB();
    dma_o<25088, 6144>(rX, vx4[2], sx, stg);
    STATS(14);
    wv[9] = trread(pa + 41984); wk[9] = trread(pa + 9216);
    SB();
    dma_o<29184, 7168>(rX, vx4[3], sx, stg);
    STATS(15);
    wv[10] = trread(pa + 43008); wk[10] = trread(pa + 10240);
    SB();
    aC = mfma(selk, ck0, aC); aC = mfma(selk, ck1, aC); aC = mfma(selv, cv0, aC); aC = mfma(selv, cv1, aC); USE(aC);
    SB();
    aK = mfma(wk[8], x[8], aK); USE(aK);
    dma_o<1179648, 24576>(rQ, vqq, kvs, kvl);
    SB();
    aV = mfma(x[8], wv[8], aV); USE(aV);
    wv[11] = trread(pa + 44032); wk[11] = trread(pa + 11264);
    SB();
    aK = mfma(wk[9], x[9], aK); USE(aK);
    dma_o<1180672, 57344>(rQ, vqq, kvs, kvl);
    SB();
    aV = mfma(x[9], wv[9], aV); USE(aV);
    wv[12] = trread(pa + 45056); wk[12] = trread(pa + 12288);
    SB();
    aK = mfma(wk[10], x[10], aK); USE(aK);
    dma_o<1228800, 25600>(rQ, vqq, kvs, kvl);
    SB();
    aV = mfma(x[10], wv[10], aV); USE(aV);
    wv[13] = trread(pa + 46080); wk[13] = trread(pa + 13312);
    SB();
    aK = mfma(wk[11], x[11], aK); USE(aK);
    dma_o<1229824, 58368>(rQ, vqq, kvs, kvl);
    SB();
    aV = mfma(x[11], wv[11], aV); USE(aV);
    wv[14] = trread(pa + 47104); wk[14] = trread(pa + 14336);
    SB();
    aK = mfma(wk[12], x[12], aK); USE(aK);
    PT_DMA(2);
    SB();
    aV = mfma(x[12], wv[12], aV); USE(aV);
    wv[15] = trread(pa + 48128); wk[15] = trread(pa + 15360);
    SB();
    aK = mfma(wk[13], x[13], aK); USE(aK);
    LOAD_PTQ();
    SB();
    aV = mfma(x[13], wv[13], aV); USE(aV);
    SB();
    aK = mfma(wk[14], x[14], aK); USE(aK);
    PT_DMA(3);
    SB();
    aV = mfma(x[14], wv[14], aV); USE(aV);
    SB();
    aK = mfma(wk[15], x[15], aK); USE(aK);
    LOAD_RES(1);
    SB();
    aV = mfma(x[15], wv[15], aV); USE(aV);
    SB();
    asm volatile("s_waitcnt vmcnt(8)" ::: "memory");
    x[16] = lds128(lds + (xr ^ 0) + 0);
    x[17] = lds128(lds + (xr ^ 32) + 0);
    x[18] = lds128(lds + (xr ^ 64) + 0);
    x[19] = lds128(lds + (xr ^ 96) + 0);
    x[20] = lds128(lds + (xr ^ 128) + 0);
    x[21] = lds128(lds + (xr ^ 160) + 0);
    x[22] = lds128(lds + (xr ^ 192) + 0);
    x[23] = lds128(lds + (xr ^ 224) + 0);
    ck0 = trread(po + 16384); ck1 = trread(po + 17408); cv0 = trread(po + 49152); cv1 = trread(po + 50176);
    asm volatile("s_barrier" ::: "memory");
    SB();
    dma_o<768, 32768>(rX, vx4[0], sx, stg);
    STATS(16);
    SB();
    dma_o<4864, 33792>(rX, vx4[1], sx, stg);
    STATS(17);
    SB();
    dma_o<8960, 34816>(rX, vx4[2], sx, stg);
    STATS(18);
    SB();
    dma_o<13056, 35840>(rX, vx4[3], sx, stg);
    STATS(19);
    SB();
    dma_o<17152, 36864>(rX, vx4[0], sx, stg);
    STATS(20);
    SB();
    dma_o<21248, 37888>(rX, vx4[1], sx, stg);
    STATS(21);
    wv[16] = trread(pa + 49152); wk[16] = trread(pa + 16384);
    SB();
    dma_o<25344, 38912>(rX, vx4[2], sx, stg);
    STATS(22);
    wv[17] = trread(pa + 50176); wk[17] = trread(pa + 17408);
    SB();
    dma_o<29440, 39936>(rX, vx4[3], sx, stg);
    STATS(23);
    wv[18] = trread(pa + 51200); wk[18] = trread(pa + 18432);
    SB();
    aC = mfma(selk, ck0, aC); aC = mfma(selk, ck1, aC); aC = mfma(selv, cv0, aC); aC = mfma(selv, cv1, aC); USE(aC);
    SB();
    aK = mfma(wk[16], x[16], aK); USE(aK);
    dma_o<0, 0>(rQ, vqq, qs, stg);
    SB();
    aV = mfma(x[16], wv[16], aV); USE(aV);
    wv[19] = trread(pa + 52224); wk[19] = trread(pa + 19456);
    SB();
    aK = mfma(wk[17], x[17], aK); USE(aK);
    dma_o<49152, 1024>(rQ, vqq, qs, stg);
    SB();
    aV = mfma(x[17], wv[17], aV); USE(aV);
    wv[20] = trread(pa + 53248); wk[20] = trread(pa + 20480);
    SB();
    aK = mfma(wk[18], x[18], aK); USE(aK);
    dma_o<98304, 2048>(rQ, vqq, qs, stg);
    SB();
    aV = mfma(x[18], wv[18], aV); USE(aV);
    wv[21] = trread(pa + 54272); wk[21] = trread(pa + 21504);
    SB();
    aK = mfma(wk[19], x[19], aK); USE(aK);
    dma_o<147456, 3072>(rQ, vqq, qs, stg);
    SB();
    aV = mfma(x[19], wv[19], aV); USE(aV);
    wv[22] = trread(pa + 55296); wk[22] = trread(pa + 22528);
    SB();
    aK = mfma(wk[20], x[20], aK); USE(aK);
    dma_o<196608, 4096>(rQ, vqq, qs, stg);
    SB();
    aV = mfma(x[20], wv[20], aV); USE(aV);
    wv[23] = trread(pa + 56320); wk[23] = trread(pa + 23552);
    asm volatile("s_waitcnt vmcnt(17)" ::: "memory");
    ck0 = trread(po + 24576); ck1 = trread(po + 25600); cv0 = trread(po + 57344); cv1 = trread(po + 58368);
    SB();
    aK = mfma(wk[21], x[21], aK); USE(aK);
    dma_o<245760, 5120>(rQ, vqq, qs, stg);
    SB();
    aV = mfma(x[21], wv[21], aV); USE(aV);
    SB();
    aK = mfma(wk[22], x[22], aK); USE(aK);
    dma_o<294912, 6144>(rQ, vqq, qs, stg);
    SB();
    aV = mfma(x[22], wv[22], aV); USE(aV);
    SB();
    aK = mfma(wk[23], x[23], aK); USE(aK);
    dma_o<344064, 7168>(rQ, vqq, qs, stg);
    SB();
    aV = mfma(x[23], wv[23], aV); USE(aV);
    SB();
    aC = mfma(selk, ck0, aC); aC = mfma(selk, ck1, aC); aC = mfma(selv, cv0, aC); aC = mfma(selv, cv1, aC); USE(aC);
    *(LAS float*)(lds + T_PART + w * 256 + lane * 4) = h ? aC[8] : aC[0];
    SB();
    asm volatile("s_waitcnt vmcnt(8)" ::: "memory");
    x[24] = lds128(lds + (xr ^ 0) + 32768);
    x[25] = lds128(lds + (xr ^ 32) + 32768);
    x[26] = lds128(lds + (xr ^ 64) + 32768);
    x[27] = lds128(lds + (xr ^ 96) + 32768);
    x[28] = lds128(lds + (xr ^ 128) + 32768);
    x[29] = lds128(lds + (xr ^ 160) + 32768);
    x[30] = lds128(lds + (xr ^ 192) + 32768);
    x[31] = lds128(lds + (xr ^ 224) + 32768);
    BAR();
    PINS();
    pt[0] = TRF(psc, 1024); pt[1] = TRF(psc + 64, 1024); pt[2] = TRF(psc + 128, 1024); pt[3] = TRF(psc + 192, 1024); ptk = trread(pa + (pn * 4 + w) * 1024);
    TABLE();
    SB();
    STATS(24);
    SB();
    STATS(25);
    SB();
    PT_STORE(0);
    STATS(26);
    SB();
    PT_STORE(1);
    STATS(27);
    SB();
    PT_STORE(2);
    STATS(28);
    SB();
    PT_STORE(3);
    STATS(29);
    wv[24] = trread(pa + 57344); wk[24] = trread(pa + 24576);
    SB();
    PT_STORE(4);
    STATS(30);
    wv[25] = trread(pa + 58368); wk[25] = trread(pa + 25600);
    SB();
    PT_STORE(5);
    STATS(31);
    wv[26] = trread(pa + 59392); wk[26] = trread(pa + 26624);
    SB();
    aK = mfma(wk[24], x[24], aK); USE(aK);
    SB();
    aV = mfma(x[24], wv[24], aV); USE(aV);
    wv[27] = trread(pa + 60416); wk[27] = trread(pa + 27648);
    LNSTATS();
    SB();
    aK = mfma(wk[25], x[25], aK); USE(aK);
    SB();
    aV = mfma(x[25], wv[25], aV); USE(aV);
    wv[28] = trread(pa + 61440); wk[28] = trread(pa + 28672);
    SB();
    aK = mfma(wk[26], x[26], aK); USE(aK);
    SB();
    aV = mfma(x[26], wv[26], aV); USE(aV);
    wv[29] = trread(pa + 62464); wk[29] = trread(pa + 29696);
    SB();
    aK = mfma(wk[27], x[27], aK); USE(aK);
    SB();
    aV = mfma(x[27], wv[27], aV); USE(aV);
    wv[30] = trread(pa + 63488); wk[30] = trread(pa + 30720);
    aK = mfma(gk, xa, aK); aV = mfma(xa, gv, aV); USE(aK); USE(aV);
    SB();
    aK = mfma(wk[28], x[28], aK); USE(aK);
    SB();
    aV = mfma(x[28], wv[28], aV); USE(aV);
    wv[31] = trread(pa + 64512); wk[31] = trread(pa + 31744);
    SB();
    aK = mfma(wk[29], x[29], aK); USE(aK);
    SB();
    aV = mfma(x[29], wv[29], aV); USE(aV);
    RS4();
    SB();
    aK = mfma(wk[30], x[30], aK); USE(aK);
    SB();
    aV = mfma(x[30], wv[30], aV); USE(aV);
    SB();
    aK = mfma(wk[31], x[31], aK); USE(aK);
    SB();
    aV = mfma(x[31], wv[31], aV); USE(aV);
    SB();
    const u64 tag = ((u64)(unsigned)__builtin_amdgcn_readfirstlane(e[1]) << 32 | (unsigned)__builtin_amdgcn_readfirstlane(e[0])) + 1;
    tag_lo = (unsigned)tag; tag_hi = (unsigned)(tag >> 32);
  }
#pragma unroll 1
  for (;;) {
    v4u qa, qb;
    {
      ST(2);
      CTX();
      const unsigned skv = OFF_KV + L * 4 * MB + (j * 64 + gtb) * 4096;
      LAS unsigned char* pb = lds + LB + trb;
      constexpr int QD = 5;
      v4u wq[32];
      {
        v4u ka, kb, v0, v1;
#pragma unroll
        for (int i = 0; i < 4; ++i) { ka[i] = pk(aK[2 * i] * rstd, aK[2 * i + 1] * rstd); kb[i] = pk(aK[8 + 2 * i] * rstd, aK[9 + 2 * i] * rstd); }
        st128c(ka, rW, v16, skv); st128c(kb, rW, v16 + 1024, skv);
        SB();
#pragma unroll
        for (int i = 0; i < 4; ++i) {
          v0[i] = pk(aV[2 * i] * rs4[i >> 1][(2 * i) & 3], aV[2 * i + 1] * rs4[i >> 1][(2 * i + 1) & 3]);
          v1[i] = pk(aV[8 + 2 * i] * rs4[2 + (i >> 1)][(2 * i) & 3], aV[9 + 2 * i] * rs4[2 + (i >> 1)][(2 * i + 1) & 3]);
        }
        st128c(v0, rW, v16 + 2048, skv); st128c(v1, rW, v16 + 3072, skv);
        asm volatile("s_waitcnt vmcnt(4)\n\ts_barrier" ::: "memory");
        post(rW, 0, OFF_HELLO + b * 8, tid == 64 && L == 0, tlo, thi);
#pragma unroll
        for (int s = 0; s < QD; ++s) wq[s] = trread(pb + s * 1024);
        LAS unsigned char* own = lds + LB + (8 + w) * TS + v16;
        *(LAS v4u*)(own) = v0; *(LAS v4u*)(own + 1040) = v1; *(LAS v4u*)(own + 2080) = ka; *(LAS v4u*)(own + 3104) = kb;
      }
      SB();
      f32x16 aQ = zero16();
      v4u gq;
#pragma unroll
      for (int s = 0; s < 32; ++s) {
        aQ = mfma(wq[s], x[s], aQ);
        USE(aQ);
        if (s + QD < 32) wq[s + QD] = trread(pb + (s + QD) * 1024);
        if (s == 28) gq = lds128(lds + T_AUGQ + 1024 + tok * 16);
        SB();
        if (s == QFLAG) {
          ST(22);
          asm volatile("s_waitcnt vmcnt(1)" ::: "memory");
          post(rW, 0, OFF_FKV + ((L * 256 + cu) * 4 + w) * 128, lane == 0, tlo, thi);
          ST(23);
          if (WARM & 1) dmac4j(rW, v16, skv, junk);
          SB();
        }
      }
      const int src = pn - 2 + (w >> 1);
      const bool ok = src >= 0;
      const unsigned fo = OFF_FKV + ((L * 256 + (ok ? src : 0) * 16 + j) * 4 + (ok ? 2 * (w & 1) : 0)) * 128, fv = (lane & 1) * 128u;
      v2u fl = ald64<0>(rW, fv, fo);
      if (L == 0) {
        f32x16 aC = zero16();
#pragma unroll
        for (int i = 0; i < 8; ++i) aC = mfma(ones, trread(pb + w * 8192 + i * 1024), aC);
        *(LAS float*)(lds + T_AUGQ + 1536 + w * 128 + tok * 4) = aC[0];
        BAR();
        LAS float* pp = (LAS float*)(lds + T_AUGQ + 1536 + tok * 4);
        gq = aug_entry((pp[0] + pp[32]) + (pp[64] + pp[96]), 0);
        if (w == 0) *(LAS v4u*)(lds + T_AUGQ + 1024 + tok * 16) = gq;
      } else {
        BAR();
      }
      aQ = mfma(gq, xa, aQ);
      unsigned sk0 = OFF_KV + L * 4 * MB + j * 64 * 4096 + (ok ? (pn * 4 - 8 + 2 * w) * 4096 : 0), sk1 = sk0 + (ok ? 4096 : 0), dst = LB + 2 * w * TS;
      unsigned vk = ok ? v16 : (unsigned)(h * 512), vv = ok ? v16 : 0x80000000u;
      asm volatile("" : "+s"(sk0), "+s"(sk1), "+s"(dst)); asm volatile("" : "+v"(vk), "+v"(vv));
      const float r2 = rstd * (0.25f * 1.4426950408889634f);
#pragma unroll
      for (int i = 0; i < 4; ++i) { qa[i] = pk(aQ[2 * i] * r2, aQ[2 * i + 1] * r2); qb[i] = pk(aQ[8 + 2 * i] * r2, aQ[9 + 2 * i] * r2); }
      USE(qa); USE(qb);
      ST(3);
      WAITV(0, fl);
      poll_rest(fl, rW, fv, fo, tlo, thi);
      ST(4);
      dmac_o<0, 2080>(rW, vk, sk0, dst); dmac_o<0, TS + 2080>(rW, vk, sk1, dst);
      dmac_o<2048, 0>(rW, vv, sk0, dst); dmac_o<2048, TS>(rW, vv, sk1, dst);
      dmac_o<3072, 1040>(rW, vv, sk0, dst); dmac_o<3072, TS + 1040>(rW, vv, sk1, dst);
      dmac_o<1024, 3104>(rW, vk, sk0, dst); dmac_o<1024, TS + 3104>(rW, vk, sk1, dst);
    }
    {
      CTX();
      v2u hl = ald64<0>(rW, lane * 8u, OFF_HELLO + w * 512);
      unsigned w1s = OFF_W1T + (j * 112 + w * 16) * 1024, w1l = LA + w * 16384, w2s = OFF_W2T + (ng * 224 + w * 56) * 1024;
      asm volatile("" : "+s"(w1s), "+s"(w1l), "+s"(w2s));
      if (w == 3 && lane < 24) *(LAS v4u*)(lds + LB + (lane >> 1) * TS + 1024 + (lane & 1) * 1040) = (pn * 4 - 8 + (lane >> 1) >= 0) ? ones : v4u{0, 0, 0, 0};
      unsigned pm[8];
      const int dq = tok - 4 * h;
#pragma unroll
      for (int r = 0; r < 16; r += 2) { const int i0 = (r & 3) + 8 * (r >> 2); pm[r >> 1] = (i0 <= dq ? 0xffffu : 0u) | (i0 + 1 <= dq ? 0xffff0000u : 0u); asm volatile("" : "+v"(pm[r >> 1])); }
      const unsigned so = OFF_O + L * 2 * MB + gtb * 32768 + j * 2048;
      LAS unsigned char* tb = lds + LB + w * TS;
      WAITV(0, hl);
      poll_rest(hl, rW, lane * 8, OFF_HELLO + w * 512, tlo, thi);
      BAR();
      post(rW, 0, OFF_EPOCH, b == 0 && tid == 0 && L == 0, tlo, thi);
      ST(19);
      v4u oa = attn_unit<0>(tb + 2080 + v16, tb + ((lane & 16) ? ((lane & 11) ? ZOFF : 1024u) : v16), qa, pm, dq,
        [&](auto D_) { constexpr int d = D_.value;
          if constexpr (d < 4) { dmac2<d * 4096>(rW, v16, w1s, w1l); dmac2<d * 4096 + 2048>(rW, v16, w1s, w1l); } else w2_load4_if0<4 * (d - 4)>(L, rW, v16, w2s); });
      st128c(oa, rW, v16, so);
      ST(20);
      if (L == 0) dmac4(rW, v16, OFF_W1T + (j * 112 + 96 + w * 4) * 1024, LC + w * 4096);
      SB();
      v4u ob = attn_unit<1>(tb + 3104 + v16, tb + ((lane & 16) ? v16 : ((lane & 11) ? ZOFF : 1024u)), qb, pm, dq,
        [&](auto D_) { constexpr int d = D_.value; if constexpr (d < 7) w2_load4_if0<16 + 4 * d>(L, rW, v16, w2s); });
      st128c(ob, rW, v16 + 1024, so);
      ST(5);
      WAIT_VM0();
      if (WARM & 2) { dmac(rW, v16, so, junk); dmac(rW, v16, so + 1024, junk); }
      BAR();
      post(rW, lane * 128, OFF_MBX + ((0 * 2 + L) * 256 + pn * 16) * 128 + j * 8, tid < 16, tlo, thi);
      ST(6);
    }
#define FIN_PRE(FFN, XOFF, STOFF, HOP) \
      f32x4 r4[4]; \
      _Pragma("unroll") for (int t = 0; t < 2; ++t) { const v4u r = res[t]; \
        r4[2 * t] = f32x4{bflo(r[0]), bfhi(r[0]), bflo(r[1]), bfhi(r[1])}; r4[2 * t + 1] = f32x4{bflo(r[2]), bfhi(r[2]), bflo(r[3]), bfhi(r[3])}; \
        if (FFN) { r4[2 * t] += ldsf4(lds + T_B2 + fc * 32); r4[2 * t + 1] += ldsf4(lds + T_B2 + fc * 32 + 16); } } \
      unsigned rd = LB + fc * 1024 + (((w * 8 + ft) ^ ((fc & 1) << 3)) * 16), wr0 = LB + w * 8192 + v16, wr1 = LB + w * 8192 + (v16 ^ 128); \
      unsigned vo = (fc >> 1) * 1024 + ((fc & 1) * 32 + ft) * 16, vs = (lane & 24) ? 0x80000000u : (unsigned)(h * 2048 + ft * 8), vn = tid < 16 ? lane * 128u : 0x80000000u; \
      unsigned xo_ = (XOFF) + (pn2 * 4 + th * 2) * 32768 + ng * 4096 + w * 128, xo1_ = xo_ + 32768, sto_ = (STOFF) + (pn2 * 4 + th * 2) * 2048 + ng * 256 + w * 64; \
      unsigned nto_ = OFF_MBX + ((HOP) * 256 + pn2 * 16) * 128 + j2 * 8; \
      asm volatile("" : "+v"(r4[0]), "+v"(r4[1]), "+v"(r4[2]), "+v"(r4[3]), "+v"(rd), "+v"(wr0), "+v"(wr1), "+v"(vo), "+v"(vs), "+v"(vn)); \
      asm volatile("" : "+s"(xo_), "+s"(xo1_), "+s"(sto_), "+s"(nto_));
#define FIN_POST(FFN) \
      { \
        f32x4 lo[2][4], hi[2][4]; \
        _Pragma("unroll") for (int t = 0; t < 2; ++t) _Pragma("unroll") for (int q = 0; q < 4; ++q) { \
          lo[t][q] = ldsf4(lds + rd + t * 32768 + q * 8192); hi[t][q] = ldsf4(lds + rd + t * 32768 + q * 8192 + 512); } \
        SB(); \
        _Pragma("unroll") for (int t = 0; t < 2; ++t) { \
          const f32x4 l4 = ((r4[2 * t] + lo[t][0]) + lo[t][1]) + (lo[t][2] + lo[t][3]), h4 = ((r4[2 * t + 1] + hi[t][0]) + hi[t][1]) + (hi[t][2] + hi[t][3]); \
          res[t] = v4u{pk(l4[0], l4[1]), pk(l4[2], l4[3]), pk(h4[0], h4[1]), pk(h4[2], h4[3])}; \
          if (!(FFN)) { st128c(res[t], rW, vo, t ? xo1_ : xo_); SB(); } \
        } \
        if (FFN) { \
          ST(17); \
          if (L == 1) {     \
            v4u o0, o1; const int sl = (((lane & 7) << 3) | (lane >> 3)) * 4; \
            _Pragma("unroll") for (int i = 0; i < 4; ++i) { o0[i] = __builtin_amdgcn_ds_bpermute(sl, res[0][i]); o1[i] = __builtin_amdgcn_ds_bpermute(sl, res[1][i]); } \
            asm volatile("buffer_store_dwordx4 %0, %2, %3, %4 offen" OUTAUX "\n\tbuffer_store_dwordx4 %1, %2, %3, %5 offen" OUTAUX "\n\ts_endpgm" \
                         ::"v"(o0), "v"(o1), "v"(vout), "s"(rOut), "s"(sout), "s"(sout + 32768) : "memory"); \
          } \
          st128c(res[0], rW, vo, xo_); st128c(res[1], rW, vo, xo1_); \
        } \
        SB(); \
        float S[2], Q[2]; \
        const bf16x2 one2 = asb2(0x3f803f80u); \
        _Pragma("unroll") for (int t = 0; t < 2; ++t) { S[t] = 0.f; Q[t] = 0.f; \
          _Pragma("unroll") for (int i = 0; i < 4; ++i) { const bf16x2 u = asb2(res[t][i]); S[t] = __builtin_amdgcn_fdot2_f32_bf16(u, one2, S[t], false); Q[t] = __builtin_amdgcn_fdot2_f32_bf16(u, u, Q[t], false); } } \
        _Pragma("unroll") for (int t = 0; t < 2; ++t) { S[t] = x8_add(S[t]); Q[t] = x8_add(Q[t]); } \
          \
        auto rS = __builtin_amdgcn_permlane32_swap(asu(S[0]), asu(S[1]), false, false); auto rQ_ = __builtin_amdgcn_permlane32_swap(asu(Q[0]), asu(Q[1]), false, false); \
        const float Sx = x16_add(asf(rS[0]) + asf(rS[1])), Qx = x16_add(asf(rQ_[0]) + asf(rQ_[1])); \
        st64c(v2u{asu(Sx), asu(Qx)}, rW, vs, sto_); \
        asm volatile("s_waitcnt vmcnt(0)\n\ts_barrier" ::: "memory"); \
        st64c(v2u{tlo, thi}, rW, vn, nto_); \
      }
    {
      CTX();
      v4u wo[8], xo[16];
      const unsigned wos = OFF_WOT + (ng * 64 + w * 16) * 1024;
      static_for<0, 8>([&](auto I_) { constexpr int i = I_.value; wo[i] = ALDF(rW, v16, wos, 2 * i); });
      if (L == 0) {
        static_for<0, 8>([&](auto I_) { constexpr int i = I_.value; acc_load<224 + 4 * i, (i & 1) * 2048 + 1024>(rW, v16, wos + (i >> 1) * 4096); });
      }
      v2u pf = ald64<0>(rW, w == 0 ? (lane & 15) * 8u : 0x80000000u, OFF_MBX + ((0 * 2 + L) * 256 + cu2) * 128);
      if (L == 0) {
        const unsigned onesk = (lane & 16) ? 0u : 0x3f803f80u, onesv = onesk ^ 0x3f803f80u;
        const v4u selk = {onesk, onesk, onesk, onesk}, selv = {onesv, onesv, onesv, onesv};
        f32x16 aC = zero16();
#pragma unroll
        for (int i = 0; i < 16; ++i) aC = mfma(selk, lds128(lds + LA + w * 16384 + v16 + i * 1024), aC);
#pragma unroll
        for (int i = 0; i < 4; ++i) aC = mfma(selv, lds128(lds + LC + w * 4096 + v16 + i * 1024), aC);
        *(LAS float*)(lds + T_PART + w * 256 + lane * 4) = h ? aC[8] : aC[0];
      }
      unsigned sxo = OFF_O + L * 2 * MB + (pn2 * 4 + th * 2) * 32768 + w * 8192;
      asm volatile("" : "+s"(sxo));
      FIN_PRE(false, OFF_X1 + L * 2 * MB, OFF_ST + L * 131072, 2 + L)
      WAITV(0, pf);
      if (w == 0) poll_rest(pf, rW, (lane & 15) * 8, OFF_MBX + ((0 * 2 + L) * 256 + cu2) * 128, tlo, thi);
      BAR();
      ST(7);
      constexpr int WO_W = WINO;
#define LDO(i) ALDF(rW, v16, sxo + ((i) >> 3) * 32768, (i) & 7)
      static_for<0, WO_W>([&](auto I_) { constexpr int i = I_.value; xo[i] = LDO(i); });
      SB();
      f32x16 a0 = zero16(), a1, a2 = zero16(), a3;
      static_for<0, 16>([&](auto I_) {
        constexpr int i = I_.value, s = i & 7;
        asm volatile("s_waitcnt vmcnt(%2)" : "+v"(xo[i]), "+v"(wo[s]) : "n"(i + WO_W <= 16 ? WO_W - 1 : 15 - i));
        if constexpr (i < 8) w2_mfma<56 + s, s == 0>(a1, xo[i]); else w2_mfma<56 + s, s == 0>(a3, xo[i]);
        if constexpr (i + WO_W < 16) xo[i + WO_W] = LDO(i + WO_W);
        SB();
        if constexpr (i < 8) { a0 = mfma(wo[s], xo[i], a0); USE(a0); } else { a2 = mfma(wo[s], xo[i], a2); USE(a2); }
        if constexpr (i >= 11 && i < 15) { red_write(lds, wr0, wr1, 0, a0, i - 11); red_write(lds, wr0, wr1, 4096, a1, i - 11); }
        SB();
      });
      asm volatile("s_nop 10" : "+v"(a3));
#pragma unroll
      for (int g = 0; g < 4; ++g) red_write(lds, wr0, wr1, 32768 + 4096, a3, g);
#pragma unroll
      for (int g = 0; g < 4; ++g) red_write(lds, wr0, wr1, 32768, a2, g);
      BAR();
      ST(8);
      const rsrc_t rOut = rW; const unsigned vout = 0, sout = 0;
      FIN_POST(false)
      ST(9);
    }
    {
      CTX();
      dmac4(rW, v16, OFF_W1T + (j * 112 + 64 + w * 8) * 1024, LB + w * 8192); dmac4(rW, v16, OFF_W1T + (j * 112 + 68 + w * 8) * 1024, LB + w * 8192 + 4096);
      const unsigned vh = (lane & 16) ? LC + ZOFF : LC + ((lane & 15) + 32 * h) * 16;
      unsigned w2s = OFF_W2T + (ng * 224 + w * 56) * 1024;
      unsigned bt0 = 0, bt1 = 0;
      if (L == 0) {
        const rsrc_t rB1 = mk_rsrc(P->b1, 3584);
        asm volatile("buffer_load_ushort %0, %2, %4, %5 offen\n\tbuffer_load_ushort %1, %3, %4, %5 offen offset:128" : "=&v"(bt0), "=&v"(bt1)
                     : "v"(lane * 2), "v"((h ? 32 + (tok & 15) : tok) * 2), "s"(rB1), "s"(j * 224));
        w2_load_range<44, 56>(rW, v16, w2s);
      }
      v2u pf = ald64<0>(rW, w == 0 ? (lane & 15) * 8u : 0x80000000u, OFF_MBX + ((1 * 2 + L) * 256 + cu) * 128);
      if (L == 0) {
        asm volatile("s_waitcnt vmcnt(13)" : "+v"(bt0), "+v"(bt1)::"memory");
        f32x16 aC = zero16();
#pragma unroll
        for (int i = 0; i < 8; ++i) aC = mfma(ones, lds128(lds + LB + w * 8192 + v16 + i * 1024), aC);
        *(LAS float*)(lds + T_PART + 1024 + w * 128 + tok * 4) = aC[0];
      }
      unsigned sh = OFF_H + L * 7 * MB + gtb * 114688 + j * 7168, kvs = OFF_WQT + (j * 96 + w * 16) * 1024, kvl = LA + w * 16384, sx = OFF_X1 + L * 2 * MB + gtb * 32768;
      unsigned fps = OFF_FH + (((L * 256 + pn * 16 + (w >> 1)) * 4 + (j >> 2)) * 4) * 128 + ((j & 3) * 2 + (w & 1)) * 8, fcs = OFF_FH + ((L * 256 + cu2) * 4 + w) * 4 * 128;
      unsigned hs0 = OFF_H + L * 7 * MB + (pn2 * 4 + th * 2) * 114688 + w * 28672, hs1 = hs0 + 114688;
      asm volatile("" : "+s"(sh), "+s"(kvs), "+s"(kvl), "+s"(sx), "+s"(w2s), "+s"(fps), "+s"(fcs), "+s"(hs0), "+s"(hs1));
      const unsigned fpv = lane < 8 ? lane * 4096u : 0x80000000u, fcv = (lane & 7) * 8u;
      v4u xh[56]; v2u fl[4];
      WAITV(0, pf);
      if (w == 0) poll_rest(pf, rW, (lane & 15) * 8, OFF_MBX + ((1 * 2 + L) * 256 + cu) * 128, tlo, thi);
      BAR();
      ST(11);
      v4u x[32], xa;
      v2u st[4];
      static_for<0, 4>([&](auto S_) { constexpr int s = S_.value; x[s] = ALDF(rW, v16, sx, s); });
      static_for<0, 4>([&](auto I_) { constexpr int i = I_.value; st[i] = ald64<i * 256>(rW, (unsigned)(h * 1024 + tok * 8), OFF_ST + L * 131072 + gtb * 2048); });
      static_for<4, WIN>([&](auto S_) { constexpr int s = S_.value; x[s] = ALDF(rW, v16, sx, s); });
      if (L == 0) {
        LAS float* p1 = (LAS float*)(lds + T_PART + h * 512 + tok * 4);
        LAS float* p2 = (LAS float*)(lds + T_PART + (h ? 128 + (tok & 15) * 4 : 1024 + tok * 4));
        const int st2 = h ? 64 : 32;
        float c2 = (p2[0] + p2[st2]) + (p2[2 * st2] + p2[3 * st2]);
        if (h) c2 += (p2[16] + p2[80]) + (p2[144] + p2[208]);
        *(LAS v4u*)(lds + T_AUG1 + lane * 16) = aug_entry(p1[0] + p1[64], bt0);
        *(LAS v4u*)(lds + T_AUG1 + 1024 + lane * 16) = aug_entry(c2, bt1);
      }
      f32x16 a0 = zero16(), a1 = zero16();
      LAS unsigned char* pa = lds + LA + v16;
      LAS unsigned char* pb = lds + LB + v16;
      v4u f0[32], f1[32], f2[64], g1[4];
#pragma unroll
      for (int s = 0; s < KD; ++s) { f1[s] = lds128(pa + 32768 + s * 1024); f0[s] = lds128(pa + s * 1024); }
      SB();
      float rs, sd;
      static_for<0, 32>([&](auto S_) {
        constexpr int s = S_.value;
        WAITV((s + WIN <= 32 ? WIN - 1 : 31 - s) + (s < 4 ? 4 : 0), x[s]);
        a0 = mfma(f0[s], x[s], a0);
        USE(a0);
        if constexpr (s + WIN < 32) x[s + WIN] = ALDF(rW, v16, sx, s + WIN);
        a1 = mfma(f1[s], x[s], a1);
        USE(a1);
        if constexpr (s + KD < 32) { f1[s + KD] = lds128(pa + 32768 + (s + KD) * 1024); f0[s + KD] = lds128(pa + (s + KD) * 1024); }
        if constexpr (s == 29) { f2[1] = lds128(pb + 1024); f2[0] = lds128(pb); }
        if constexpr (s == 30) { f2[3] = lds128(pb + 3072); f2[2] = lds128(pb + 2048); }
        if constexpr (s == 31) { g1[3] = lds128(lds + T_AUG1 + 1536 + tok * 16); g1[2] = lds128(lds + T_AUG1 + 1024 + tok * 16); }
        if constexpr (s == 6) {
          asm volatile("" : "+v"(st[0]), "+v"(st[1]), "+v"(st[2]), "+v"(st[3]));
          ln_stats((asf(st[0][0]) + asf(st[1][0])) + (asf(st[2][0]) + asf(st[3][0])), (asf(st[0][1]) + asf(st[1][1])) + (asf(st[2][1]) + asf(st[3][1])), h, rs, sd, xa);
          USE(xa); USE(rs); USE(sd);
          g1[1] = lds128(lds + T_AUG1 + 512 + tok * 16); g1[0] = lds128(lds + T_AUG1 + tok * 16);
        }
        if constexpr (s == 10) { a0 = mfma(g1[0], xa, a0); a1 = mfma(g1[1], xa, a1); USE(a0); USE(a1); }
        SB();
      });
      ST(12);
      asm volatile("s_barrier" ::: "memory");
      SB();
      const float c0 = (-2.f * 1.4426950408889634f * 0.7978845608028654f) * rs, c1 = c0 * 0.044715f * rs * rs;
      f32x16 a2, a3;
      auto rdf2 = [&](auto M_) {
        constexpr int m = M_.value;
        if constexpr (m < 32) f2[m] = lds128(pb + m * 1024);
        else if constexpr (m < 64) f2[m] = lds128(lds + vh + (m & 1) * 256 + ((m - 32) >> 1) * 1024);
      };
      auto mstep = [&](auto M_) {
        constexpr int m = M_.value;
        if constexpr (m < 64) {
          SB();
          if constexpr (m < 32) { a2 = mfma(f2[m], x[m], a2); USE(a2); } else { a3 = mfma(f2[m], x[m - 32], a3); USE(a3); }
          if constexpr ((m & 1) == 0) { rdf2(IC(m + 5)); rdf2(IC(m + 4)); }
          SB();
        }
      };
      a2 = mfma(g1[2], xa, zero16()); USE(a2);
      asm volatile("s_nop 7");
      SB();
      v4u ho;
      auto group = [&](const f32x16& a, auto T_, auto G_, auto M_) {
        constexpr int t = T_.value, g = G_.value, M = M_.value;
        const float v0 = a[4 * g], v1 = a[4 * g + 1], v2 = a[4 * g + 2], v3 = a[4 * g + 3];
        float u0, u1, u2, u3;
        mstep(IC(M));
        asm volatile("v_mul_f32 %0, %4, %4\n\tv_mul_f32 %1, %5, %5\n\tv_mul_f32 %2, %6, %6\n\tv_mul_f32 %3, %7, %7"
                     : "=&v"(u0), "=&v"(u1), "=&v"(u2), "=&v"(u3) : "v"(v0), "v"(v1), "v"(v2), "v"(v3));
        mstep(IC(M + 1));
        asm volatile("v_fma_f32 %0, %0, %5, %6\n\tv_fma_f32 %1, %1, %5, %6\n\tv_fma_f32 %2, %2, %5, %6\n\tv_fma_f32 %3, %3, %5, %6\n\tv_mul_f32 %0, %0, %4"
                     : "+v"(u0), "+v"(u1), "+v"(u2), "+v"(u3) : "v"(v0), "v"(c1), "v"(c0));
        mstep(IC(M + 2));
        asm volatile("v_mul_f32 %0, %0, %3\n\tv_mul_f32 %1, %1, %4\n\tv_mul_f32 %2, %2, %5" : "+v"(u1), "+v"(u2), "+v"(u3) : "v"(v1), "v"(v2), "v"(v3));
        mstep(IC(M + 3));
        asm volatile("v_exp_f32 %0, %0\n\tv_exp_f32 %1, %1\n\tv_exp_f32 %2, %2" : "+v"(u0), "+v"(u1), "+v"(u2));
        mstep(IC(M + 4));
        asm volatile("v_exp_f32 %2, %2\n\tv_fma_f32 %0, %0, %3, %3\n\tv_fma_f32 %1, %1, %3, %3" : "+v"(u0), "+v"(u1), "+v"(u3) : "v"(sd));
        mstep(IC(M + 5));
        asm volatile("v_fma_f32 %2, %2, %4, %4\n\tv_fma_f32 %3, %3, %4, %4\n\tv_rcp_f32 %0, %0\n\tv_rcp_f32 %1, %1" : "+v"(u0), "+v"(u1), "+v"(u2), "+v"(u3) : "v"(sd));
        mstep(IC(M + 6));
        asm volatile("v_rcp_f32 %1, %1\n\tv_rcp_f32 %2, %2\n\tv_mul_f32 %0, %0, %3" : "+v"(u0), "+v"(u2), "+v"(u3) : "v"(v0));
        mstep(IC(M + 7));
        unsigned o0, o1;
        asm volatile("v_mul_f32 %3, %3, %6\n\tv_mul_f32 %4, %4, %7\n\tv_mul_f32 %5, %5, %8\n\tv_cvt_pk_bf16_f32 %0, %2, %3\n\tv_cvt_pk_bf16_f32 %1, %4, %5"
                     : "=&v"(o0), "=&v"(o1), "+v"(u0), "+v"(u1), "+v"(u2), "+v"(u3) : "v"(v1), "v"(v2), "v"(v3));
        ho[2 * (g & 1)] = o0; ho[2 * (g & 1) + 1] = o1;
        if constexpr (g & 1) st128c(ho, rW, v16 + ((2 * t + (g >> 1)) & 3) * 1024, sh + ((2 * t + (g >> 1)) >> 2) * 4096);
        SB();
      };
      auto pkfrag = [&](const f32x16& a, auto F_, auto H_) {
        constexpr int f = F_.value, hf = H_.value;
        const f32x2 k0 = {c0, c0}, k1 = {c1, c1}, ks = {sd, sd};
        f32x2 v[4], u[4];
#pragma unroll
        for (int i = 0; i < 4; ++i) v[i] = f32x2{a[8 * hf + 2 * i], a[8 * hf + 2 * i + 1]};
#pragma unroll
        for (int i = 0; i < 4; ++i) u[i] = v[i] * v[i];
#pragma unroll
        for (int i = 0; i < 4; ++i) u[i] = __builtin_elementwise_fma(u[i], k1, k0);
#pragma unroll
        for (int i = 0; i < 4; ++i) u[i] = u[i] * v[i];
#pragma unroll
        for (int i = 0; i < 4; ++i) u[i] = f32x2{ex2(u[i][0]), ex2(u[i][1])};
#pragma unroll
        for (int i = 0; i < 4; ++i) u[i] = __builtin_elementwise_fma(u[i], ks, ks);
#pragma unroll
        for (int i = 0; i < 4; ++i) u[i] = f32x2{__builtin_amdgcn_rcpf(u[i][0]), __builtin_amdgcn_rcpf(u[i][1])};
        v4u o;
#pragma unroll
        for (int i = 0; i < 4; ++i) { u[i] = u[i] * v[i]; o[i] = pk(u[i][0], u[i][1]); }
        st128c(o, rW, v16 + (f & 3) * 1024, sh + (f >> 2) * 4096);
      };
      f32x16 y0, y1, y2, y3;
      group(a0, IC(0), IC(0), IC(0));
      dmac4_if0<0>(L, rW, v16, kvs, kvl);
      SB();
      group(a0, IC(0), IC(1), IC(8));
      dmac4_if0<4096>(L, rW, v16, kvs, kvl);
      SB();
      group(a0, IC(0), IC(2), IC(16));
      a3 = mfma(g1[3], xa, zero16()); USE(a3);
      dmac4_if0<8192>(L, rW, v16, kvs, kvl);
      SB();
      group(a0, IC(0), IC(3), IC(24));
      dmac4_if0<12288>(L, rW, v16, kvs, kvl);
      SB();
      group(a1, IC(1), IC(0), IC(32));
      SB();
      group(a1, IC(1), IC(1), IC(40));
      SB();
      group(a1, IC(1), IC(2), IC(48));
      SB();
      group(a1, IC(1), IC(3), IC(56));
      SB();
      asm volatile("s_waitcnt vmcnt(1)" ::: "memory");
      st64c(v2u{tlo, thi}, rW, fpv + 0, fps);
      SB();
      pkfrag(a2, IC(4), IC(0));
      SB();
      SB();
      pkfrag(a2, IC(5), IC(1));
      SB();
      SB();
      pkfrag(a3, IC(6), IC(0));
      SB();
      ST(13);
      fl[0] = ald64<0>(rW, fcv, fcs);
      asm volatile("s_waitcnt vmcnt(1)" ::: "memory");
      st64c(v2u{tlo, thi}, rW, fpv + 128, fps);
      WAITV(1, fl[0]); poll_rest(fl[0], rW, fcv + 0, fcs, tlo, thi);
      ST(15);
      xh[0] = ald128<0>(rW, v16, hs0 + 0);
      xh[1] = ald128<1024>(rW, v16, hs0 + 0);
      xh[2] = ald128<2048>(rW, v16, hs0 + 0);
      xh[3] = ald128<3072>(rW, v16, hs0 + 4096);
      xh[4] = ald128<0>(rW, v16, hs0 + 8192);
      xh[5] = ald128<1024>(rW, v16, hs0 + 8192);
      xh[6] = ald128<2048>(rW, v16, hs0 + 12288);
      xh[7] = ald128<3072>(rW, v16, hs0 + 12288);
      xh[8] = ald128<0>(rW, v16, hs0 + 16384);
      FIN_PRE(true, OFF_X2, OFF_ST + 2 * 131072, 6)
      unsigned vout = fc * 1024 + ft * 16, sout = ((pn2 * 4 + th * 2) * 32 + w * 8) * 1024 + ng * 128; const rsrc_t rOut = mk_rsrc(P->out, 2048 * 512 * 2);
      asm volatile("" : "+s"(sout) : "s"(rOut)); asm volatile("" : "+v"(vout));
      SB();
      w2_mfma_w<8, 0, true>(y0, xh[0]);
      xh[9] = ald128<1024>(rW, v16, hs0 + 20480);
      w2_mfma<1, true>(y1, xh[0]);
      SB();
      w2_mfma_w<8, 2, false>(y0, xh[1]);
      xh[10] = ald128<2048>(rW, v16, hs0 + 20480);
      w2_mfma<3, false>(y1, xh[1]);
      SB();
      w2_mfma_w<8, 4, false>(y0, xh[2]);
      xh[11] = ald128<3072>(rW, v16, hs0 + 20480);
      w2_mfma<5, false>(y1, xh[2]);
      SB();
      w2_mfma_w<8, 14, false>(y0, xh[3]);
      xh[12] = ald128<0>(rW, v16, hs1 + 0);
      w2_mfma<15, false>(y1, xh[3]);
      SB();
      w2_mfma_w<8, 16, false>(y0, xh[4]);
      xh[13] = ald128<1024>(rW, v16, hs1 + 0);
      w2_mfma<17, false>(y1, xh[4]);
      SB();
      w2_mfma_w<8, 18, false>(y0, xh[5]);
      xh[14] = ald128<2048>(rW, v16, hs1 + 0);
      w2_mfma<19, false>(y1, xh[5]);
      SB();
      w2_mfma_w<8, 28, false>(y0, xh[6]);
      xh[15] = ald128<3072>(rW, v16, hs1 + 4096);
      w2_mfma<29, false>(y1, xh[6]);
      SB();
      w2_mfma_w<8, 30, false>(y0, xh[7]);
      xh[16] = ald128<0>(rW, v16, hs1 + 8192);
      w2_mfma<31, false>(y1, xh[7]);
      SB();
      w2_mfma_w<8, 32, false>(y0, xh[8]);
      fl[1] = ald64<128>(rW, fcv, fcs);
      xh[17] = ald128<1024>(rW, v16, hs1 + 8192);
      w2_mfma<33, false>(y1, xh[8]);
      SB();
      w2_mfma_w<9, 42, false>(y0, xh[9]);
      xh[18] = ald128<2048>(rW, v16, hs1 + 12288);
      w2_mfma<43, false>(y1, xh[9]);
      SB();
      w2_mfma_w<9, 44, false>(y0, xh[10]);
      xh[19] = ald128<3072>(rW, v16, hs1 + 12288);
      w2_mfma<45, false>(y1, xh[10]);
      SB();
      w2_mfma_w<9, 46, false>(y0, xh[11]);
      xh[20] = ald128<0>(rW, v16, hs1 + 16384);
      w2_mfma<47, false>(y1, xh[11]);
      SB();
      w2_mfma_w<9, 0, true>(y2, xh[12]);
      xh[21] = ald128<1024>(rW, v16, hs1 + 20480);
      w2_mfma<1, true>(y3, xh[12]);
      SB();
      w2_mfma_w<9, 2, false>(y2, xh[13]);
      xh[22] = ald128<2048>(rW, v16, hs1 + 20480);
      w2_mfma<3, false>(y3, xh[13]);
      SB();
      w2_mfma_w<9, 4, false>(y2, xh[14]);
      xh[23] = ald128<3072>(rW, v16, hs1 + 20480);
      w2_mfma<5, false>(y3, xh[14]);
      SB();
      w2_mfma_w<9, 14, false>(y2, xh[15]);
      WAITV(7, fl[1]); poll_rest(fl[1], rW, fcv + 128, fcs, tlo, thi);
      xh[24] = ald128<3072>(rW, v16, hs0 + 0);
      w2_mfma<15, false>(y3, xh[15]);
      SB();
      w2_mfma_w<9, 16, false>(y2, xh[16]);
      xh[25] = ald128<0>(rW, v16, hs0 + 4096);
      w2_mfma<17, false>(y3, xh[16]);
      SB();
      w2_mfma_w<8, 18, false>(y2, xh[17]);
      xh[26] = ald128<1024>(rW, v16, hs0 + 4096);
      w2_mfma<19, false>(y3, xh[17]);
      SB();
      w2_mfma_w<8, 28, false>(y2, xh[18]);
      xh[27] = ald128<2048>(rW, v16, hs0 + 4096);
      w2_mfma<29, false>(y3, xh[18]);
      SB();
      w2_mfma_w<8, 30, false>(y2, xh[19]);
      xh[28] = ald128<2048>(rW, v16, hs0 + 8192);
      w2_mfma<31, false>(y3, xh[19]);
      SB();
      w2_mfma_w<8, 32, false>(y2, xh[20]);
      xh[29] = ald128<3072>(rW, v16, hs0 + 8192);
      w2_mfma<33, false>(y3, xh[20]);
      SB();
      w2_mfma_w<8, 42, false>(y2, xh[21]);
      xh[30] = ald128<0>(rW, v16, hs0 + 12288);
      w2_mfma<43, false>(y3, xh[21]);
      SB();
      w2_mfma_w<8, 44, false>(y2, xh[22]);
      xh[31] = ald128<1024>(rW, v16, hs0 + 12288);
      w2_mfma<45, false>(y3, xh[22]);
      SB();
      w2_mfma_w<8, 46, false>(y2, xh[23]);
      xh[32] = ald128<1024>(rW, v16, hs0 + 16384);
      w2_mfma<47, false>(y3, xh[23]);
      SB();
      w2_mfma_w<8, 6, false>(y0, xh[24]);
      xh[33] = ald128<2048>(rW, v16, hs0 + 16384);
      w2_mfma<7, false>(y1, xh[24]);
      SB();
      w2_mfma_w<8, 8, false>(y0, xh[25]);
      xh[34] = ald128<3072>(rW, v16, hs0 + 16384);
      w2_mfma<9, false>(y1, xh[25]);
      SB();
      w2_mfma_w<8, 10, false>(y0, xh[26]);
      xh[35] = ald128<0>(rW, v16, hs0 + 20480);
      w2_mfma<11, false>(y1, xh[26]);
      SB();
      w2_mfma_w<8, 12, false>(y0, xh[27]);
      xh[36] = ald128<0>(rW, v16, hs0 + 24576);
      w2_mfma<13, false>(y1, xh[27]);
      SB();
      w2_mfma_w<8, 20, false>(y0, xh[28]);
      xh[37] = ald128<1024>(rW, v16, hs0 + 24576);
      w2_mfma<21, false>(y1, xh[28]);
      SB();
      w2_mfma_w<8, 22, false>(y0, xh[29]);
      xh[38] = ald128<2048>(rW, v16, hs0 + 24576);
      w2_mfma<23, false>(y1, xh[29]);
      SB();
      w2_mfma_w<8, 24, false>(y0, xh[30]);
      xh[39] = ald128<3072>(rW, v16, hs0 + 24576);
      w2_mfma<25, false>(y1, xh[30]);
      SB();
      w2_mfma_w<8, 26, false>(y0, xh[31]);
      xh[40] = ald128<3072>(rW, v16, hs1 + 0);
      w2_mfma<27, false>(y1, xh[31]);
      SB();
      w2_mfma_w<8, 34, false>(y0, xh[32]);
      xh[41] = ald128<0>(rW, v16, hs1 + 4096);
      w2_mfma<35, false>(y1, xh[32]);
      SB();
      w2_mfma_w<8, 36, false>(y0, xh[33]);
      xh[42] = ald128<1024>(rW, v16, hs1 + 4096);
      w2_mfma<37, false>(y1, xh[33]);
      SB();
      w2_mfma_w<8, 38, false>(y0, xh[34]);
      xh[43] = ald128<2048>(rW, v16, hs1 + 4096);
      w2_mfma<39, false>(y1, xh[34]);
      SB();
      w2_mfma_w<8, 40, false>(y0, xh[35]);
      xh[44] = ald128<2048>(rW, v16, hs1 + 8192);
      w2_mfma<41, false>(y1, xh[35]);
      SB();
      w2_mfma_w<8, 48, false>(y0, xh[36]);
      xh[45] = ald128<3072>(rW, v16, hs1 + 8192);
      w2_mfma<49, false>(y1, xh[36]);
      SB();
      w2_mfma_w<8, 50, false>(y0, xh[37]);
      xh[46] = ald128<0>(rW, v16, hs1 + 12288);
      w2_mfma<51, false>(y1, xh[37]);
      SB();
      w2_mfma_w<8, 52, false>(y0, xh[38]);
      xh[47] = ald128<1024>(rW, v16, hs1 + 12288);
      w2_mfma<53, false>(y1, xh[38]);
      SB();
      w2_mfma_w<8, 54, false>(y0, xh[39]);
      xh[48] = ald128<1024>(rW, v16, hs1 + 16384);
      w2_mfma<55, false>(y1, xh[39]);
      SB();
      w2_mfma_w<8, 6, false>(y2, xh[40]);
      xh[49] = ald128<2048>(rW, v16, hs1 + 16384);
      w2_mfma<7, false>(y3, xh[40]);
      SB();
      w2_mfma_w<8, 8, false>(y2, xh[41]);
      asm volatile("s_barrier" ::: "memory");
      xh[50] = ald128<3072>(rW, v16, hs1 + 16384);
      w2_mfma<9, false>(y3, xh[41]);
      SB();
      w2_mfma_w<8, 10, false>(y2, xh[42]);
      xh[51] = ald128<0>(rW, v16, hs1 + 20480);
      w2_mfma<11, false>(y3, xh[42]);
      SB();
      w2_mfma_w<8, 12, false>(y2, xh[43]);
      xh[52] = ald128<0>(rW, v16, hs1 + 24576);
      w2_mfma<13, false>(y3, xh[43]);
      SB();
      w2_mfma_w<8, 20, false>(y2, xh[44]);
      xh[53] = ald128<1024>(rW, v16, hs1 + 24576);
      w2_mfma<21, false>(y3, xh[44]);
      red_write(lds, wr0, wr1, 0, y0, 0);
      SB();
      w2_mfma_w<8, 22, false>(y2, xh[45]);
      xh[54] = ald128<2048>(rW, v16, hs1 + 24576);
      w2_mfma<23, false>(y3, xh[45]);
      red_write(lds, wr0, wr1, 0, y0, 1);
      SB();
      w2_mfma_w<8, 24, false>(y2, xh[46]);
      xh[55] = ald128<3072>(rW, v16, hs1 + 24576);
      w2_mfma<25, false>(y3, xh[46]);
      red_write(lds, wr0, wr1, 0, y0, 2);
      SB();
      w2_mfma_w<8, 26, false>(y2, xh[47]);
      w2_mfma<27, false>(y3, xh[47]);
      red_write(lds, wr0, wr1, 0, y0, 3);
      SB();
      w2_mfma_w<7, 34, false>(y2, xh[48]);
      w2_mfma<35, false>(y3, xh[48]);
      red_write(lds, wr0, wr1, 4096, y1, 0);
      SB();
      w2_mfma_w<6, 36, false>(y2, xh[49]);
      w2_mfma<37, false>(y3, xh[49]);
      red_write(lds, wr0, wr1, 4096, y1, 1);
      SB();
      w2_mfma_w<5, 38, false>(y2, xh[50]);
      w2_mfma<39, false>(y3, xh[50]);
      red_write(lds, wr0, wr1, 4096, y1, 2);
      SB();
      w2_mfma_w<4, 40, false>(y2, xh[51]);
      w2_mfma<41, false>(y3, xh[51]);
      red_write(lds, wr0, wr1, 4096, y1, 3);
      SB();
      w2_mfma_w<3, 48, false>(y2, xh[52]);
      w2_mfma<49, false>(y3, xh[52]);
      SB();
      w2_mfma_w<2, 50, false>(y2, xh[53]);
      w2_mfma<51, false>(y3, xh[53]);
      SB();
      w2_mfma_w<1, 52, false>(y2, xh[54]);
      w2_mfma<53, false>(y3, xh[54]);
      SB();
      w2_mfma_w<0, 54, false>(y2, xh[55]);
      w2_mfma<55, false>(y3, xh[55]);
      SB();
      asm volatile("s_nop 10" : "+v"(y2), "+v"(y3));
#pragma unroll
      for (int g = 0; g < 4; ++g) red_write(lds, wr0, wr1, 32768, y2, g);
#pragma unroll
      for (int g = 0; g < 4; ++g) red_write(lds, wr0, wr1, 32768 + 4096, y3, g);
      BAR();
      ST(16);
      FIN_POST(true)
      dmac4(rW, v16, OFF_WQT + (j * 96 + 64 + w * 8) * 1024, LB + w * 8192); dmac4(rW, v16, OFF_WQT + (j * 96 + 68 + w * 8) * 1024, LB + w * 8192 + 4096);
      ST(18);
    }
    {
      CTX();
      if (w == 0) poll(rW, (lane & 15) * 8, OFF_MBX + ((3 * 2 + 0) * 256 + cu) * 128, tlo, thi);
      BAR();
      L = 1; ST(1);
      aK = zero16(); aV = zero16();
      const unsigned sx = OFF_X2 + gtb * 32768;
      v2u st[4];
      static_for<0, 4>([&](auto S_) { constexpr int s = S_.value; x[s] = ALDF(rW, v16, sx, s); });
      static_for<0, 4>([&](auto I_) { constexpr int i = I_.value; st[i] = ald64<i * 256>(rW, (unsigned)(h * 1024 + tok * 8), OFF_ST + 2 * 131072 + gtb * 2048); });
      static_for<4, WIN>([&](auto S_) { constexpr int s = S_.value; x[s] = ALDF(rW, v16, sx, s); });
      LAS unsigned char* pa = lds + LA + v16;
      v4u wk[32], wv[32], gk, gv;
#pragma unroll
      for (int s = 0; s < KD; ++s) { wv[s] = lds128(pa + 32768 + s * 1024); wk[s] = lds128(pa + s * 1024); }
      SB();
      static_for<0, 32>([&](auto S_) {
        constexpr int s = S_.value;
        WAITV((s + WIN <= 32 ? WIN - 1 : 31 - s) + (s < 4 ? 4 : 0), x[s]);
        aK = mfma(wk[s], x[s], aK);
        USE(aK);
        if constexpr (s + WIN < 32) x[s + WIN] = ALDF(rW, v16, sx, s + WIN);
        aV = mfma(x[s], wv[s], aV);
        USE(aV);
        if constexpr (s + KD < 32) { wv[s + KD] = lds128(pa + 32768 + (s + KD) * 1024); wk[s + KD] = lds128(pa + (s + KD) * 1024); }
        if constexpr (s == 6) {
          asm volatile("" : "+v"(st[0]), "+v"(st[1]), "+v"(st[2]), "+v"(st[3]));
          float sd;
          ln_stats((asf(st[0][0]) + asf(st[1][0])) + (asf(st[2][0]) + asf(st[3][0])), (asf(st[0][1]) + asf(st[1][1])) + (asf(st[2][1]) + asf(st[3][1])), h, rstd, sd, xa);
          USE(xa);
          *(LAS float*)(lds + T_STAT + w * 128 + tok * 4) = rstd;
          gv = lds128(lds + T_AUGQ + 512 + tok * 16); gk = lds128(lds + T_AUGQ + tok * 16);
        }
        if constexpr (s == 10) { aK = mfma(gk, xa, aK); aV = mfma(xa, gv, aV); USE(aK); USE(aV); }
        if constexpr (s == 30) {
#pragma unroll
          for (int g = 0; g < 4; ++g) rs4[g] = ldsf4(lds + T_STAT + w * 128 + (8 * g + 4 * h) * 4);
        }
        SB();
      });
    }
  }
}
extern "C" __global__ void fused_tail() { asm volatile(".Lfused_end:"); }
}

extern "C" void kernel_launch(void* const* d_in, const int* in_sizes, int n_in, void* d_out, int out_size, void* d_ws, size_t ws_size, hipStream_t stream) {
  Params p;
  p.x = (const unsigned char*)d_in[0]; p.wqkv = (const unsigned char*)d_in[1]; p.wo = (const unsigned char*)d_in[2];
  p.w1 = (const unsigned char*)d_in[4]; p.b1 = (const unsigned char*)d_in[5]; p.w2 = (const unsigned char*)d_in[6]; p.b2 = (const unsigned char*)d_in[7];
  p.out = (unsigned char*)d_out; p.ws = (unsigned char*)d_ws;
  (void)hipFuncSetAttribute((const void*)fused, hipFuncAttributeMaxDynamicSharedMemorySize, LDS_BYTES);
  fused<<<256, 256, LDS_BYTES, stream>>>(p);
}
```
